```python
import torch

def _softmax(x, axis=-1):
    return torch.softmax(x, dim=axis)
def _gelu(x):
    return torch.nn.functional.gelu(x, approximate="tanh")
def _silu(x):
    return torch.nn.functional.silu(x)
def _sigmoid(x):
    return torch.sigmoid(x)
def _layernorm(x, axis=-1, eps=1e-5):
    m = x.mean(dim=axis, keepdim=True)
    v = x.var(dim=axis, keepdim=True, unbiased=False)
    return (x - m) / torch.sqrt(v + eps)
def _scatter_add(tgt, idx, src, axis=0):
    return tgt.clone().index_add_(axis, idx, src)
def _segment_sum(src, seg, n):
    out = torch.zeros((n,) + src.shape[1:], dtype=src.dtype)
    return out.index_add_(0, seg, src)
def _pad(x, pw):
    flat = tuple(v for pair in reversed(pw) for v in pair)
    return torch.nn.functional.pad(x, flat)

kernel_name = 'recurrent_scan__spectral_gfnet__dense_activation_relu__prompt'

def setup_inputs():

    _w = (lambda a: (lambda t: t.to(torch.bfloat16) if t.is_floating_point() else t)(torch.as_tensor(a)))
    def _A_setup():
        B, S, D = 1, 2048, 512
        Df = D // 2 + 1
        return {
            "x": _w((lambda *s: torch.randn(*s, dtype=torch.bfloat16) * 0.05)(B, S, D)),
            "w_r": _w((lambda *s: torch.randn(*s, dtype=torch.bfloat16) * 0.1)(S, Df) * 0.02),
            "w_i": _w((lambda *s: torch.randn(*s, dtype=torch.bfloat16) * 0.1)(S, Df) * 0.02),
        }
    _A_in = _A_setup()

    def _B_setup():
        B, S, D, Dff = 1, 2048, 512, 1792
        return {
            "x": _w((lambda *s: torch.randn(*s, dtype=torch.bfloat16) * 0.05)(B, S, D)),
            "w1": _w((lambda *s: torch.randn(*s, dtype=torch.bfloat16) * 0.05)(D, Dff)),
            "w2": _w((lambda *s: torch.randn(*s, dtype=torch.bfloat16) * 0.05)(Dff, D)),
        }
    _B_in = _B_setup()

    return {
        "A_x": _A_in["x"],
        "A_w_r": _A_in["w_r"],
        "A_w_i": _A_in["w_i"],
        "B_x": _B_in["x"],
        "B_w1": _B_in["w1"],
        "B_w2": _B_in["w2"],
    }

def reference(A_x, A_w_r, A_w_i, B_x, B_w1, B_w2):
    def _A_ref(x, w_r, w_i):
        B, S, D = x.shape
        X = (lambda x, axes=(-2,-1): torch.fft.rfft2(x.float(), dim=axes))(x, axes=(-2, -1))
        W = w_r + 1j * w_i
        X = X * W
        y = (lambda x, s, axes=(-2,-1): torch.fft.irfft2(x, s=s, dim=axes).to(torch.bfloat16))(X, (S, D), axes=(-2, -1))
        return y

    def _B_ref(x, w1, w2):
        h = torch.einsum("bsd,df->bsf", x, w1)
        zero = torch.zeros_like(h)
        h = torch.maximum(h, zero)
        return torch.einsum("bsf,fd->bsd", h, w2)

    x = A_x
    xn = _layernorm(x, axis=-1)
    s = _A_ref(xn, A_w_r, A_w_i)
    g = _silu(_B_ref(xn, B_w1, B_w2))
    return x + s * g
```

```cpp
#include <hip/hip_runtime.h>
#include <stdint.h>
#pragma clang diagnostic ignored "-Wint-to-pointer-cast"

typedef short s16x4 __attribute__((ext_vector_type(4)));
typedef short s16x8 __attribute__((ext_vector_type(8)));
typedef __bf16 bf16x8 __attribute__((ext_vector_type(8)));
typedef __bf16 bf16x2 __attribute__((ext_vector_type(2)));
typedef float f32x4 __attribute__((ext_vector_type(4)));
typedef float f32x2 __attribute__((ext_vector_type(2)));
typedef unsigned u32x4 __attribute__((ext_vector_type(4)));
typedef unsigned u32x2 __attribute__((ext_vector_type(2)));
#define LAS __attribute__((address_space(3)))
#define GAS __attribute__((address_space(1)))
#define DEVI __device__ __forceinline__

constexpr size_t WS_H = 0;
constexpr size_t WS_X1T = 8u << 20;
constexpr size_t WS_WT = 12u << 20;
constexpr size_t WS_ZT = 16u << 20;
constexpr size_t WS_FLAG = 20u << 20;
constexpr size_t WS_TS = 24u << 20;

#define STAMP(k, id) do { } while (0)

DEVI unsigned pk2(float a, float b) {
    f32x2 f = {a, b};
    return __builtin_bit_cast(unsigned, __builtin_convertvector(f, bf16x2));
}
DEVI float bf_round(float a) { return __builtin_bit_cast(float, pk2(a, a) << 16); }
DEVI float bf_lo(unsigned u) { return __builtin_bit_cast(float, u << 16); }
DEVI float bf_hi(unsigned u) { return __builtin_bit_cast(float, u & 0xffff0000u); }
DEVI f32x4 mfma(u32x4 a, u32x4 b, f32x4 c) {
    return __builtin_amdgcn_mfma_f32_16x16x32_bf16(__builtin_bit_cast(bf16x8, a), __builtin_bit_cast(bf16x8, b), c, 0, 0, 0);
}
DEVI float dot2(unsigned a, unsigned b, float c) {
    return __builtin_amdgcn_fdot2_f32_bf16(__builtin_bit_cast(bf16x2, a), __builtin_bit_cast(bf16x2, b), c, false);
}
DEVI u32x2 trread(unsigned addr) {
    s16x4 v = __builtin_amdgcn_ds_read_tr16_b64_v4i16((LAS s16x4*)addr);
    return __builtin_bit_cast(u32x2, v);
}
DEVI u32x4 trread2(unsigned a0, unsigned a1) {
    u32x2 lo = trread(a0), hi = trread(a1);
    u32x4 r = {lo[0], lo[1], hi[0], hi[1]};
    return r;
}
DEVI u32x4 lds_rd16(unsigned addr) { return *(LAS u32x4*)addr; }
DEVI void lds_wr16(unsigned addr, u32x4 v) { *(LAS u32x4*)addr = v; }
DEVI void lds_wr8(unsigned addr, u32x2 v) { *(LAS u32x2*)addr = v; }
DEVI float cosr(float rev) { return __builtin_amdgcn_cosf(rev); }
DEVI float sinr(float rev) { return __builtin_amdgcn_sinf(rev); }
DEVI void barrier() { asm volatile("s_barrier" ::: "memory"); }
#define WAIT_VM(n) asm volatile("s_waitcnt vmcnt(%0)" ::"n"(n) : "memory")
#define WAIT_LGKM0() asm volatile("s_waitcnt lgkmcnt(0)" ::: "memory")

DEVI void dma16(unsigned voff, const void* sbase, unsigned dst) {
    unsigned keep;
    asm volatile("s_mov_b32 %0, m0\n\ts_mov_b32 m0, %3\n\ts_nop 0\n\tglobal_load_lds_dwordx4 %1, %2\n\ts_mov_b32 m0, %0"
                 : "=&s"(keep) : "v"(voff), "s"(sbase), "s"(dst) : "memory");
}

constexpr int K1_CS = 147456, K1_ST = K1_CS + 512, K1_LDS = K1_ST + 1024;

template <int NH>
DEVI void k1_consumer(const char* __restrict__ x, char* ws, int P, int w, int lane, int ii0, int fg0) {
    constexpr int NF = 9 - NH;
    constexpr int PITCH = NH * 32;
    constexpr unsigned FOFF = 512 * PITCH;
    const int n = lane & 15, g = lane >> 4, q = n >> 2, p = n & 3;
    const int t0 = 2 * P + (w >> 1);
    const unsigned tl0 = 8 * (n >> 2) + (n & 3);
    const unsigned xoff = (32u * (32 * (w & 1) + tl0) + t0) * 1024u + g * 16;
    const GAS char* xb = (const GAS char*)x;
    u32x4 xr[8][2];
#pragma unroll
    for (int ks = 0; ks < 8; ++ks) {
        xr[ks][0] = *(const GAS u32x4*)(xb + (xoff + ks * 64));
        xr[ks][1] = *(const GAS u32x4*)(xb + (xoff + 131072u + ks * 64));
    }
    const unsigned swz = (NH == 8) ? (q | ((g & 1) << 2)) : ((q >> 1) | ((g & 1) << 1));
    const unsigned abase = (8 * g + q) * PITCH + swz * 32 + p * 8;
    const unsigned fbase = FOFF + lane * 16;
    f32x4 acc[2][9];
#pragma unroll
    for (int i = 0; i < 9; ++i) { acc[0][i] = f32x4{0, 0, 0, 0}; acc[1][i] = f32x4{0, 0, 0, 0}; }
    float sx[2] = {0, 0}, sq[2] = {0, 0};
    u32x4 A[9];
    auto rdA = [&](int ks, int db) -> u32x4 {
        if (db < NH) { const unsigned a = (abase ^ (db << 5)) + ks * 32 * PITCH; return trread2(a, a + 4 * PITCH); }
        else return lds_rd16(fbase + ((db - NH) * 16 + ks) * 1024);
    };
    STAMP(0, 1);
    barrier();
    STAMP(0, 2);
#pragma unroll
    for (int db = 0; db < 9; ++db) A[db] = rdA(0, db);
#pragma unroll
    for (int ks = 0; ks < 16; ++ks) {
        if ((ks & 1) && ks < 15) barrier();
        const u32x4 x0 = xr[ks & 7][0], x1 = xr[ks & 7][1];
#pragma unroll
        for (int db = 0; db < 9; ++db) {
            if (db < NH) { acc[0][db] = mfma(A[db], x0, acc[0][db]); acc[1][db] = mfma(A[db], x1, acc[1][db]); }
            else { acc[0][db] = mfma(x0, A[db], acc[0][db]); acc[1][db] = mfma(x1, A[db], acc[1][db]); }
            if (ks < 15) A[db] = rdA(ks + 1, db);
            if (db < 4) {
                sx[0] = dot2(x0[db], 0x3f803f80u, sx[0]); sq[0] = dot2(x0[db], x0[db], sq[0]);
                sx[1] = dot2(x1[db], 0x3f803f80u, sx[1]); sq[1] = dot2(x1[db], x1[db], sq[1]);
            }
            __builtin_amdgcn_sched_barrier(0);
        }
        if (ks < 8) {
            xr[ks][0] = *(const GAS u32x4*)(xb + (xoff + (ks + 8) * 64));
            xr[ks][1] = *(const GAS u32x4*)(xb + (xoff + 131072u + (ks + 8) * 64));
        }
    }
    STAMP(0, 3);
    float mb[2], rr[2];
#pragma unroll
    for (int tt = 0; tt < 2; ++tt) {
        float a = sx[tt], b = sq[tt];
        a += __shfl_xor(a, 16); b += __shfl_xor(b, 16);
        a += __shfl_xor(a, 32); b += __shfl_xor(b, 32);
        const float mean = a * (1.0f / 512.0f);
        const float var = fmaxf(b * (1.0f / 512.0f) - mean * mean, 0.0f);
        mb[tt] = bf_round(mean);
        const float den = bf_round(__builtin_sqrtf(bf_round(bf_round(var) + 1e-5f)));
        rr[tt] = 1.0f / den;
        if (g == 0) lds_wr8(K1_ST + (w * 32 + tt * 16 + n) * 8, u32x2{__builtin_bit_cast(unsigned, mb[tt]), __builtin_bit_cast(unsigned, rr[tt])});
    }
    WAIT_LGKM0();
    barrier();
    GAS char* wsb = (GAS char*)ws;
    const int tile64 = 2 * P + (w >> 1);
#pragma unroll
    for (int i2 = 0; i2 < NH / 2; ++i2) {
        const f32x4 c0 = *(LAS f32x4*)(unsigned)(K1_CS + (32 * i2 + 4 * g) * 4), c1 = *(LAS f32x4*)(unsigned)(K1_CS + (32 * i2 + 16 + 4 * g) * 4);
#pragma unroll
        for (int tt = 0; tt < 2; ++tt) {
            float v[8];
#pragma unroll
            for (int r = 0; r < 4; ++r) {
                v[r] = fmaxf(rr[tt] * (acc[tt][2 * i2][r] - mb[tt] * c0[r]), 0.0f);
                v[4 + r] = fmaxf(rr[tt] * (acc[tt][2 * i2 + 1][r] - mb[tt] * c1[r]), 0.0f);
            }
            const u32x4 o = {pk2(v[0], v[1]), pk2(v[2], v[3]), pk2(v[4], v[5]), pk2(v[6], v[7])};
            *(GAS u32x4*)(wsb + WS_H + (size_t)(((tile64 * 56 + ii0 + i2) * 4 + 2 * (w & 1) + tt) * 1024 + lane * 16)) = o;
        }
    }
    f32x4 st[2][2];
#pragma unroll
    for (int tt = 0; tt < 2; ++tt) {
        st[tt][0] = *(LAS f32x4*)(unsigned)(K1_ST + (w * 32 + tt * 16 + 4 * g) * 8);
        st[tt][1] = *(LAS f32x4*)(unsigned)(K1_ST + (w * 32 + tt * 16 + 4 * g) * 8 + 16);
    }
#pragma unroll
    for (int f = 0; f < NF; ++f) {
        const int c = 16 * (fg0 + f) + n;
        const float cs = (c == 0) ? 512.0f : 0.0f;
        float v[8];
#pragma unroll
        for (int tt = 0; tt < 2; ++tt)
#pragma unroll
            for (int r = 0; r < 4; ++r) v[4 * tt + r] = st[tt][r >> 1][2 * (r & 1) + 1] * (acc[tt][NH + f][r] - st[tt][r >> 1][2 * (r & 1)] * cs);
        const u32x4 o = {pk2(v[0], v[1]), pk2(v[2], v[3]), pk2(v[4], v[5]), pk2(v[6], v[7])};
        *(GAS u32x4*)(wsb + WS_X1T + (size_t)(unsigned)(c * 4096 + (128 * P + 32 * w + 8 * g) * 2)) = o;
    }
    STAMP(0, 4);
}

template <int NH>
DEVI void k1_loader(const char* __restrict__ w1, const unsigned short* wr, const unsigned short* wi,
                    char* ws, int b, int u, int lane, int col0, int fg0) {
    constexpr int NF = 9 - NH;
    constexpr int PITCH = NH * 32;
    constexpr unsigned FOFF = 512 * PITCH;
    constexpr int PPS = NH / 2;
    const int n = lane & 15, g = lane >> 4, q = n >> 2, p = n & 3;
    unsigned voff[2];
    if (NH == 8) {
        const int r4 = lane >> 4, p16 = lane & 15;
#pragma unroll
        for (int j = 0; j < 2; ++j) voff[j] = r4 * 3584 + (((p16 >> 1) ^ (r4 | (j << 2))) * 32) + (p16 & 1) * 16;
    } else {
        const int r8 = lane >> 3, p16 = lane & 7;
#pragma unroll
        for (int j = 0; j < 2; ++j) voff[j] = r8 * 3584 + (((p16 >> 1) ^ (((r8 >> 1) & 1) | (j << 1))) * 32) + (p16 & 1) * 16;
    }
    const char* wb = w1 + col0 * 2;
    auto issue = [&](int s) {
#pragma unroll
        for (int j = 0; j < PPS; ++j) {
            const int pi = (NH == 8) ? (16 * s + 4 * u + j) : (8 * s + 2 * u + j);
            const int rows = (NH == 8) ? 4 : 8;
            dma16(voff[(NH == 8) ? ((j >> 1) & 1) : (j & 1)], wb + (size_t)(pi * rows) * 3584, pi * 1024);
        }
    };
    issue(0); issue(1);
    const int rs = b & 63, cq = b >> 6;
    const int jv = rs & 3, ja = (rs >> 2) & 1, jt = (rs >> 3) & 1, jg = rs >> 4;
    unsigned short wv[2][4][2], wsp;
    {
        const GAS unsigned short* wrg = (const GAS unsigned short*)wr;
        const GAS unsigned short* wig = (const GAS unsigned short*)wi;
#pragma unroll
        for (int e = 0; e < 2; ++e)
#pragma unroll
            for (int r = 0; r < 4; ++r) {
                const unsigned k1 = 16 * jv + 8 * ja + 2 * u + e + 64 * (16 * jt + 4 * jg + r);
                const unsigned idx = k1 * 257 + 64 * cq + lane;
                wv[e][r][0] = wrg[idx]; wv[e][r][1] = wig[idx];
            }
        const int unit = 2 * b + ((lane >> 3) & 1), reg = (lane >> 1) & 3;
        const unsigned k1 = 16 * (unit >> 7) + (unit & 15) + 64 * (16 * ((unit >> 6) & 1) + 4 * ((unit >> 4) & 3) + reg);
        wsp = ((lane & 1) ? wig : wrg)[k1 * 257 + 256];
    }
    asm volatile("" ::: "memory");
#pragma unroll
    for (int i = 0; i < NF * 2; ++i) {
        const int bi = u + 4 * i;
        const int ks0 = bi / NF, fgl = bi - ks0 * NF;
        const int c = 16 * (fg0 + fgl) + n;
        const int k2 = (c == 1) ? 256 : (c >> 1), part = (c == 1) ? 0 : (c & 1);
        const int idx0 = (k2 * (32 * ks0 + 8 * g) + 128 * part) & 511;
        const float x0 = (float)idx0 * (1.0f / 512.0f), st = (float)k2 * (1.0f / 512.0f);
        float v[8];
#pragma unroll
        for (int j = 0; j < 8; ++j) v[j] = cosr(x0 + (float)j * st);
        const u32x4 o = {pk2(v[0], v[1]), pk2(v[2], v[3]), pk2(v[4], v[5]), pk2(v[6], v[7])};
        const unsigned sg = (k2 & 1) ? 0x80008000u : 0u;
        const u32x4 o2 = {o[0] ^ sg, o[1] ^ sg, o[2] ^ sg, o[3] ^ sg};
        lds_wr16(FOFF + (fgl * 16 + ks0) * 1024 + lane * 16, o);
        lds_wr16(FOFF + (fgl * 16 + ks0 + 8) * 1024 + lane * 16, o2);
    }
#pragma unroll
    for (int s = 2; s < 8; ++s) issue(s);
    STAMP(0, 1);
    constexpr int NG = NH / 4;
    const unsigned swz = (NH == 8) ? (q | ((g & 1) << 2)) : ((q >> 1) | ((g & 1) << 1));
    const unsigned abase = (8 * g + q) * PITCH + swz * 32 + p * 8;
    float cs[NG];
#pragma unroll
    for (int i = 0; i < NG; ++i) cs[i] = 0.0f;
#pragma unroll
    for (int s = 0; s < 8; ++s) {
        if (s == 0) WAIT_VM(7 * PPS + 17);
        else if (s == 1) WAIT_VM(6 * PPS + 17);
        else if (s == 2) WAIT_VM(5 * PPS);
        else if (s == 3) WAIT_VM(4 * PPS);
        else if (s == 4) WAIT_VM(3 * PPS);
        else if (s == 5) WAIT_VM(2 * PPS);
        else if (s == 6) WAIT_VM(1 * PPS);
        else WAIT_VM(0);
        WAIT_LGKM0();
        barrier();
        if (s == 0) STAMP(0, 2);
#pragma unroll
        for (int kk = 0; kk < 2; ++kk)
#pragma unroll
            for (int i = 0; i < NG; ++i) {
                const unsigned a = (abase ^ ((NG * u + i) << 5)) + (2 * s + kk) * 32 * PITCH;
                const u32x4 f = trread2(a, a + 4 * PITCH);
#pragma unroll
                for (int e = 0; e < 4; ++e) cs[i] = dot2(f[e], 0x3f803f80u, cs[i]);
            }
    }
#pragma unroll
    for (int i = 0; i < NG; ++i) {
        float a = cs[i];
        a += __shfl_xor(a, 16); a += __shfl_xor(a, 32);
        if (g == 0) *(LAS float*)(unsigned)(K1_CS + (16 * (NG * u + i) + n) * 4) = a;
    }
    WAIT_LGKM0();
    barrier();
    GAS char* wsb = (GAS char*)ws;
#pragma unroll
    for (int e = 0; e < 2; ++e) {
        u32x4 o;
#pragma unroll
        for (int r = 0; r < 4; ++r) o[r] = (unsigned)wv[e][r][0] | ((unsigned)wv[e][r][1] << 16);
        const unsigned k2 = 64 * cq + lane;
        *(GAS u32x4*)(wsb + WS_WT + (size_t)(k2 * 8192 + ((jv * 2 + jt) * 64 + 16 * jg + 8 * ja + 2 * u + e) * 16)) = o;
    }
    if (u == 0 && lane < 16) *(GAS unsigned short*)(wsb + WS_WT + (size_t)(256 * 8192 + 2 * b * 16 + lane * 2)) = wsp;
    if (b == 0 && u == 1) *(GAS u32x4*)(wsb + WS_FLAG + lane * 16) = u32x4{0, 0, 0, 0};
    STAMP(0, 4);
}

__global__ void __launch_bounds__(512, 2) k1_kernel(const char* __restrict__ x, const unsigned short* wr,
                                                     const unsigned short* wi, const char* __restrict__ w1,
                                                     char* ws) {
    const int b = blockIdx.x, lane = threadIdx.x & 63, w = __builtin_amdgcn_readfirstlane(threadIdx.x >> 6);
    STAMP(0, 0);
    const int xcd = b & 7, slot = b >> 3;
    const int qd = xcd >> 1, hf = xcd & 1;
    const int P = 4 * qd + (slot >> 3), i = slot & 7;
    if (i < 6) {
        if (w < 4) k1_consumer<8>(x, ws, P, w, lane, 28 * hf + 4 * i, 6 * hf + i);
        else k1_loader<8>(w1, wr, wi, ws, b, w - 4, lane, 896 * hf + 128 * i, 6 * hf + i);
    } else {
        if (w < 4) k1_consumer<4>(x, ws, P, w, lane, 28 * hf + 24 + 2 * (i - 6), 12 + 5 * (2 * hf + i - 6));
        else k1_loader<4>(w1, wr, wi, ws, b, w - 4, lane, 896 * hf + 768 + 64 * (i - 6), 12 + 5 * (2 * hf + i - 6));
    }
}

constexpr int K2_Z = 65536, K2_BP = 131072, K2_ZS = K2_BP + 2 * 64 * 72, K2_SY = K2_ZS + 64 * 144, K2_LDS = K2_SY + 64;
constexpr unsigned SPIN_CAP = 1u << 22;

DEVI unsigned lds_ld_relaxed(unsigned addr) { return __hip_atomic_load((LAS unsigned*)addr, __ATOMIC_RELAXED, __HIP_MEMORY_SCOPE_WORKGROUP); }
DEVI void team_sync(unsigned cnt, unsigned target, int lane) {
    WAIT_LGKM0();
    if (lane == 0) __hip_atomic_fetch_add((LAS unsigned*)cnt, 1u, __ATOMIC_RELAXED, __HIP_MEMORY_SCOPE_WORKGROUP);
#pragma unroll 1
    for (unsigned s = 0; s < SPIN_CAP; ++s) {
        asm volatile("" ::: "memory");
        if ((unsigned)__builtin_amdgcn_readfirstlane(lds_ld_relaxed(cnt)) >= target) break;
        __builtin_amdgcn_s_sleep(1);
    }
    asm volatile("" ::: "memory");
}

#define GLOAD16(dst, voff, sbase, imm) asm volatile("global_load_dwordx4 %0, %1, %2 offset:%3" : "=v"(dst) : "v"(voff), "s"(sbase), "n"(imm) : "memory")

DEVI void k2_team1(const char* __restrict__ w2, char* ws, int tile64, int d0, int w, int lane) {
    const int n = lane & 15, g = lane >> 4, q = n >> 2, p = n & 3;
    const unsigned ring = w * 16384;
    const int r8 = lane >> 3, p16 = lane & 7;
    const unsigned voffw = r8 * 1024 + ((p16 ^ (2 * ((r8 >> 1) & 3))) * 16);
    const char* wb = w2 + d0 * 2 + (size_t)w * 32 * 1024;
    const char* hb = ws + WS_H + (size_t)(tile64 * 56 + w) * 4096;
    const unsigned voffh = lane * 16;
    u32x4 h[4][4];
    auto issue = [&](int i) {
#pragma unroll
        for (int pc = 0; pc < 4; ++pc) dma16(voffw, wb + (size_t)(i * 128 + pc * 8) * 1024, ring + (i & 3) * 4096 + pc * 1024);
        const char* hp = hb + (size_t)i * 16384;
        GLOAD16(h[i & 3][0], voffh, hp, 0); GLOAD16(h[i & 3][1], voffh, hp, 1024);
        GLOAD16(h[i & 3][2], voffh, hp, 2048); GLOAD16(h[i & 3][3], voffh, hp, 3072);
    };
#pragma unroll
    for (int i = 0; i < 4; ++i) issue(i);
    WAIT_LGKM0();
    barrier();
    f32x4 acc[4][4];
#pragma unroll
    for (int a = 0; a < 4; ++a)
#pragma unroll
        for (int t = 0; t < 4; ++t) acc[a][t] = f32x4{0, 0, 0, 0};
    const unsigned abase = ring + (4 * g + q) * 128 + (((2 * g + (q >> 1)) & 3) * 32) + p * 8;
    u32x4 A[2][4];
    auto rdA = [&](int i, u32x4 (&dst)[4]) {
#pragma unroll
        for (int db = 0; db < 4; ++db) { const unsigned a = (abase ^ (db << 5)) + (i & 3) * 4096; dst[db] = trread2(a, a + 2048); }
    };
#define T1_WAIT(N, slot) asm volatile("s_waitcnt vmcnt(%4)" : "+v"(h[slot][0]), "+v"(h[slot][1]), "+v"(h[slot][2]), "+v"(h[slot][3]) : "n"(N) : "memory")
    T1_WAIT(24, 0);
    STAMP(1, 2);
    rdA(0, A[0]);
#pragma unroll
    for (int i = 0; i < 14; ++i) {
        if (i + 1 < 14) {
            if (i + 3 < 14) T1_WAIT(16, (i + 1) & 3);
            else if (i + 2 < 14) T1_WAIT(8, (i + 1) & 3);
            else T1_WAIT(0, (i + 1) & 3);
            rdA(i + 1, A[(i + 1) & 1]);
        }
#pragma unroll
        for (int db = 0; db < 4; ++db)
#pragma unroll
            for (int t = 0; t < 4; ++t) acc[db][t] = mfma(A[i & 1][db], h[i & 3][t], acc[db][t]);
        __builtin_amdgcn_sched_barrier(0);
        if (i + 4 < 14) issue(i + 4);
    }
    STAMP(1, 3);
#pragma unroll
    for (int t = 0; t < 4; ++t)
#pragma unroll
        for (int db = 0; db < 4; ++db) *(LAS f32x4*)(ring + (t * 4 + db) * 1024 + lane * 16) = acc[db][t];
}

template <bool HAS_I>
DEVI void p2_unit(char* ws, const u32x4 (&Ur)[2][2], const u32x4 (&Ui)[2][2], const u32x4 (&Wf)[2], int zrow, int v, int lane, unsigned& sync_n,
                  const u32x4 (&C64)[2], const u32x4 (&S64)[2], const u32x4 (&N64)[2],
                  const u32x4 (&C32)[2], const u32x4 (&S32)[2], const u32x4 (&N32)[2],
                  const float (&tc)[8], const float (&tsn)[8]) {
    const int n = lane & 15, g = lane >> 4, q = n >> 2, p = n & 3;
    float br[8], bi[8];
#pragma unroll
    for (int mt = 0; mt < 2; ++mt) {
        f32x4 dr = {0, 0, 0, 0}, di = {0, 0, 0, 0};
#pragma unroll
        for (int ks = 0; ks < 2; ++ks) {
            dr = mfma(Ur[mt][ks], C64[ks], dr);
            di = mfma(Ur[mt][ks], N64[ks], di);
            if (HAS_I) { dr = mfma(Ui[mt][ks], S64[ks], dr); di = mfma(Ui[mt][ks], C64[ks], di); }
        }
#pragma unroll
        for (int r = 0; r < 4; ++r) {
            const float c = tc[4 * mt + r], s = tsn[4 * mt + r];
            br[4 * mt + r] = dr[r] * c + di[r] * s;
            bi[4 * mt + r] = di[r] * c - dr[r] * s;
        }
    }
    u32x4 Br = {pk2(br[0], br[1]), pk2(br[2], br[3]), pk2(br[4], br[5]), pk2(br[6], br[7])};
    u32x4 Bi = {pk2(bi[0], bi[1]), pk2(bi[2], bi[3]), pk2(bi[4], bi[5]), pk2(bi[6], bi[7])};
    float yr[8], yi[8];
#pragma unroll
    for (int t = 0; t < 2; ++t) {
        f32x4 dr = {0, 0, 0, 0}, di = {0, 0, 0, 0};
        dr = mfma(C32[t], Br, dr); dr = mfma(S32[t], Bi, dr);
        di = mfma(C32[t], Bi, di); di = mfma(N32[t], Br, di);
#pragma unroll
        for (int r = 0; r < 4; ++r) {
            const float wre = bf_lo(Wf[t][r]), wim = bf_hi(Wf[t][r]);
            yr[4 * t + r] = dr[r] * wre - di[r] * wim;
            yi[4 * t + r] = dr[r] * wim + di[r] * wre;
        }
    }
    u32x4 Yr = {pk2(yr[0], yr[1]), pk2(yr[2], yr[3]), pk2(yr[4], yr[5]), pk2(yr[6], yr[7])};
    u32x4 Yi = {pk2(yi[0], yi[1]), pk2(yi[2], yi[3]), pk2(yi[4], yi[5]), pk2(yi[6], yi[7])};
#pragma unroll
    for (int mt = 0; mt < 2; ++mt) {
        f32x4 dr = {0, 0, 0, 0}, di = {0, 0, 0, 0};
        dr = mfma(C32[mt], Yr, dr); dr = mfma(N32[mt], Yi, dr);
        di = mfma(C32[mt], Yi, di); di = mfma(S32[mt], Yr, di);
        float pr[4], pi[4];
#pragma unroll
        for (int r = 0; r < 4; ++r) {
            const float c = tc[4 * mt + r] * (1.0f / 2048.0f), s = tsn[4 * mt + r] * (1.0f / 2048.0f);
            pr[r] = dr[r] * c - di[r] * s;
            pi[r] = di[r] * c + dr[r] * s;
        }
        const unsigned a = K2_BP + (16 * v + n) * 72 + (16 * mt + 4 * g) * 2;
        lds_wr8(a, u32x2{pk2(pr[0], pr[1]), pk2(pr[2], pr[3])});
        lds_wr8(a + 64 * 72, u32x2{pk2(pi[0], pi[1]), pk2(pi[2], pi[3])});
    }
    sync_n += 4; team_sync(K2_SY, sync_n, lane);
#pragma unroll
    for (int nt = 0; nt < 2; ++nt) {
        f32x4 dr = {0, 0, 0, 0}, di = {0, 0, 0, 0};
#pragma unroll
        for (int ks = 0; ks < 2; ++ks) {
            const unsigned a = K2_BP + (32 * ks + 8 * g + q) * 72 + nt * 32 + p * 8;
            const u32x4 pr = trread2(a, a + 4 * 72), pi = trread2(a + 64 * 72, a + 64 * 72 + 4 * 72);
            dr = mfma(C64[ks], pr, dr); dr = mfma(N64[ks], pi, dr);
            if (HAS_I) { di = mfma(C64[ks], pi, di); di = mfma(S64[ks], pr, di); }
        }
        const unsigned a = K2_ZS + (16 * nt + n) * 144 + (16 * v + 4 * g) * 2;
        lds_wr8(a, u32x2{pk2(dr[0], dr[1]), pk2(dr[2], dr[3])});
        if (HAS_I) lds_wr8(a + 32 * 144, u32x2{pk2(di[0], di[1]), pk2(di[2], di[3])});
    }
    sync_n += 4; team_sync(K2_SY, sync_n, lane);
    {
        const __amdgpu_buffer_rsrc_t rs = __builtin_amdgcn_make_buffer_rsrc(ws + WS_ZT, 0, 512 * 4096, 0x00020000);
#pragma unroll
        for (int it = 0; it < (HAS_I ? 2 : 1); ++it) {
            const int row = 8 * v + (lane >> 3), ch = lane & 7;
            const u32x4 o = lds_rd16(K2_ZS + (it * 32 + row) * 144 + ch * 16);
            __builtin_amdgcn_raw_buffer_store_b128(o, rs, (zrow + it) * 4096 + row * 128 + ch * 16, 0, 16);
        }
    }
}

DEVI void k2_team2(char* ws, int b, int tile64, int d0, int v, int lane) {
    const int n = lane & 15, g = lane >> 4, q = n >> 2, p = n & 3;
    u32x4 Ur[2][2], Ui[2][2], Wf[2], Wg[2];
    {
        const GAS char* wsb = (const GAS char*)ws;
#pragma unroll
        for (int mt = 0; mt < 2; ++mt)
#pragma unroll
            for (int ks = 0; ks < 2; ++ks) {
                const unsigned o = 2 * b * 4096 + ((16 * mt + n) * 64 + 32 * ks + 8 * g) * 2;
                Ur[mt][ks] = *(const GAS u32x4*)(wsb + WS_X1T + (size_t)o);
                Ui[mt][ks] = *(const GAS u32x4*)(wsb + WS_X1T + (size_t)(o + 4096));
            }
#pragma unroll
        for (int t = 0; t < 2; ++t) {
            Wf[t] = *(const GAS u32x4*)(wsb + WS_WT + (size_t)(unsigned)(b * 8192 + ((v * 2 + t) * 64 + lane) * 16));
            if (b == 0) Wg[t] = *(const GAS u32x4*)(wsb + WS_WT + (size_t)(unsigned)(256 * 8192 + ((v * 2 + t) * 64 + lane) * 16));
            else Wg[t] = u32x4{0, 0, 0, 0};
        }
    }
    asm volatile("" ::: "memory");
    u32x4 C64[2], S64[2], N64[2], C32[2], S32[2], N32[2];
    float tc[8], tsn[8];
#pragma unroll
    for (int ks = 0; ks < 2; ++ks) {
        float c[8], s[8];
#pragma unroll
        for (int j = 0; j < 8; ++j) {
            const float a = (float)(((16 * v + n) * (32 * ks + 8 * g + j)) & 63) * (1.0f / 64.0f);
            c[j] = cosr(a); s[j] = sinr(a);
        }
#pragma unroll
        for (int e = 0; e < 4; ++e) { C64[ks][e] = pk2(c[2 * e], c[2 * e + 1]); S64[ks][e] = pk2(s[2 * e], s[2 * e + 1]); N64[ks][e] = S64[ks][e] ^ 0x80008000u; }
    }
#pragma unroll
    for (int t = 0; t < 2; ++t) {
        float c[8], s[8];
#pragma unroll
        for (int j = 0; j < 8; ++j) {
            const int kk = (j < 4) ? (4 * g + j) : (16 + 4 * g + j - 4);
            const float a = (float)(((16 * t + n) * kk) & 31) * (1.0f / 32.0f);
            c[j] = cosr(a); s[j] = sinr(a);
        }
#pragma unroll
        for (int e = 0; e < 4; ++e) { C32[t][e] = pk2(c[2 * e], c[2 * e + 1]); S32[t][e] = pk2(s[2 * e], s[2 * e + 1]); N32[t][e] = S32[t][e] ^ 0x80008000u; }
    }
#pragma unroll
    for (int j = 0; j < 8; ++j) {
        const float a = (float)((16 * v + n) * (16 * (j >> 2) + 4 * g + (j & 3))) * (1.0f / 2048.0f);
        tc[j] = cosr(a); tsn[j] = sinr(a);
    }
    WAIT_LGKM0();
    barrier();
    STAMP(1, 2);
    unsigned sync_n = 0;
    if (b != 0) p2_unit<true>(ws, Ur, Ui, Wf, 2 * b, v, lane, sync_n, C64, S64, N64, C32, S32, N32, tc, tsn);
    else {
        p2_unit<false>(ws, Ur, Ur, Wf, 0, v, lane, sync_n, C64, S64, N64, C32, S32, N32, tc, tsn);
        p2_unit<false>(ws, Ui, Ui, Wg, 1, v, lane, sync_n, C64, S64, N64, C32, S32, N32, tc, tsn);
    }
    WAIT_VM(0);
    STAMP(1, 3);
    GAS unsigned* flags = (GAS unsigned*)(ws + WS_FLAG);
    if (lane == 0) {
        const unsigned old = __hip_atomic_fetch_add((LAS unsigned*)(K2_SY + 4), 1u, __ATOMIC_RELAXED, __HIP_MEMORY_SCOPE_WORKGROUP);
        if (old == 3) __hip_atomic_store(flags + b, 1u, __ATOMIC_RELAXED, __HIP_MEMORY_SCOPE_AGENT);
    }
    u32x4 G[4][4];
#pragma unroll
    for (int db = 0; db < 4; ++db)
#pragma unroll
        for (int ks = 0; ks < 4; ++ks) {
            const int d = d0 + 16 * db + n;
            float vv[8];
#pragma unroll
            for (int jj = 0; jj < 4; ++jj) {
                const int k2 = 64 * v + 16 * ks + 4 * g + jj;
                const float a = (float)((k2 * d) & 511) * (1.0f / 512.0f);
                vv[2 * jj] = cosr(a); vv[2 * jj + 1] = -sinr(a);
                if (ks == 0 && jj == 0 && k2 == 0) { vv[0] = 0.5f; vv[1] = (d & 1) ? -0.5f : 0.5f; }
            }
            G[db][ks] = u32x4{pk2(vv[0], vv[1]), pk2(vv[2], vv[3]), pk2(vv[4], vv[5]), pk2(vv[6], vv[7])};
        }
    STAMP(1, 4);
    const __amdgpu_buffer_rsrc_t rz = __builtin_amdgcn_make_buffer_rsrc(ws + WS_ZT, 0, 512 * 4096, 0x00020000);
    if (v == 0) {
        const unsigned foff = lane * 16;
        const char* fb = ws + WS_FLAG;
#pragma unroll 1
        for (unsigned s = 0; s < SPIN_CAP; ++s) {
            u32x4 f;
            asm volatile("global_load_dwordx4 %0, %1, %2 sc1\n\ts_waitcnt vmcnt(0)" : "=v"(f) : "v"(foff), "s"(fb) : "memory");
            if (__all((f[0] & f[1] & f[2] & f[3]) == 1u)) break;
            __builtin_amdgcn_s_sleep(2);
        }
        asm volatile("" ::: "memory");
        if (lane == 0) __hip_atomic_store((LAS unsigned*)(K2_SY + 8), 1u, __ATOMIC_RELAXED, __HIP_MEMORY_SCOPE_WORKGROUP);
    } else {
    #pragma unroll 1
    for (unsigned s = 0; s < SPIN_CAP; ++s) {
            asm volatile("" ::: "memory");
            if ((unsigned)__builtin_amdgcn_readfirstlane(lds_ld_relaxed(K2_SY + 8)) != 0u) break;
            __builtin_amdgcn_s_sleep(2);
        }
    }
    asm volatile("" ::: "memory");
    STAMP(1, 5);
    const unsigned zreg = K2_Z + v * 16384;
    {
        const int r8 = lane >> 3, ch = lane & 7;
        u32x4 z[16];
#pragma unroll
        for (int i = 0; i < 16; ++i) z[i] = __builtin_amdgcn_raw_buffer_load_b128(rz, (128 * v + 8 * i + r8) * 4096 + tile64 * 128 + ch * 16, 0, 16);
#pragma unroll
        for (int i = 0; i < 16; ++i) lds_wr16(zreg + (8 * i + r8) * 128 + ((ch ^ (4 * ((r8 >> 1) & 1))) * 16), z[i]);
    }
    STAMP(1, 6);
    f32x4 acc[4][4];
#pragma unroll
    for (int a = 0; a < 4; ++a)
#pragma unroll
        for (int t = 0; t < 4; ++t) acc[a][t] = f32x4{0, 0, 0, 0};
#pragma unroll
    for (int ks = 0; ks < 4; ++ks)
#pragma unroll
        for (int t = 0; t < 4; ++t) {
            const unsigned a = zreg + (32 * ks + 8 * g + q) * 128 + (((t >> 1) ^ ((q >> 1) & 1)) * 64) + p * 16 + (t & 1) * 8;
            const u32x4 z = trread2(a, a + 512);
#pragma unroll
            for (int db = 0; db < 4; ++db) acc[db][t] = mfma(G[db][ks], z, acc[db][t]);
        }
#pragma unroll
    for (int t = 0; t < 4; ++t)
#pragma unroll
        for (int db = 0; db < 4; ++db) *(LAS f32x4*)(zreg + (t * 4 + db) * 1024 + lane * 16) = acc[db][t];
    STAMP(1, 7);
}

__global__ void __launch_bounds__(512, 2) k2_kernel(const char* __restrict__ x, const char* __restrict__ w2,
                                                     char* ws, char* __restrict__ out) {
    const int b = blockIdx.x, lane = threadIdx.x & 63, w = __builtin_amdgcn_readfirstlane(threadIdx.x >> 6);
    STAMP(1, 0);
    const int xcd = b & 7, slot = b >> 3;
    const int tile64 = 8 * (xcd >> 1) + (slot >> 2), d0 = 64 * (4 * (xcd & 1) + (slot & 3));
    if (threadIdx.x < 4) *(LAS unsigned*)(unsigned)(K2_SY + threadIdx.x * 4) = 0u;
    const int n = lane & 15, g = lane >> 4;
    const int T = w & 3, dp = w >> 2;
    const unsigned tok = 32u * (32 * (T >> 1) + 8 * (n >> 2) + 4 * (T & 1) + (n & 3)) + tile64;
    const unsigned ooff = tok * 1024u + (d0 + 32 * dp + 8 * g) * 2;
    const u32x4 xv = *(const GAS u32x4*)((const GAS char*)x + ooff);
    if (w < 4) k2_team1(w2, ws, tile64, d0, w, lane);
    else k2_team2(ws, b, tile64, d0, w - 4, lane);
    WAIT_LGKM0();
    barrier();
    STAMP(1, 8);
    float y[8], s[8];
#pragma unroll
    for (int j = 0; j < 8; ++j) { y[j] = 0.0f; s[j] = 0.0f; }
#pragma unroll
    for (int e = 0; e < 2; ++e) {
        const unsigned a = (T * 4 + 2 * dp + (g >> 1)) * 1024 + (n + 16 * (2 * (g & 1) + e)) * 16;
#pragma unroll
        for (int k = 0; k < 4; ++k) {
            const f32x4 py = *(LAS f32x4*)(a + k * 16384), ps = *(LAS f32x4*)(a + K2_Z + k * 16384);
#pragma unroll
            for (int r = 0; r < 4; ++r) { y[4 * e + r] += py[r]; s[4 * e + r] += ps[r]; }
        }
    }
    float o[8];
#pragma unroll
    for (int j = 0; j < 8; ++j) {
        const float yb = bf_round(y[j]);
        const float gg = bf_round(yb / (1.0f + __expf(-yb)));
        const float sb = bf_round(s[j] * (2.0f / 512.0f));
        const float xx = (j & 1) ? bf_hi(xv[j >> 1]) : bf_lo(xv[j >> 1]);
        o[j] = xx + bf_round(sb * gg);
    }
    const u32x4 ov = {pk2(o[0], o[1]), pk2(o[2], o[3]), pk2(o[4], o[5]), pk2(o[6], o[7])};
    *(GAS u32x4*)((GAS char*)out + ooff) = ov;
    STAMP(1, 9);
}

extern "C" void kernel_launch(void* const* d_in, const int* in_sizes, int n_in, void* d_out, int out_size, void* d_ws,
                              size_t ws_size, hipStream_t stream) {
    (void)in_sizes; (void)n_in; (void)out_size; (void)ws_size;
    (void)hipFuncSetAttribute((const void*)k1_kernel, hipFuncAttributeMaxDynamicSharedMemorySize, K1_LDS);
    (void)hipFuncSetAttribute((const void*)k2_kernel, hipFuncAttributeMaxDynamicSharedMemorySize, K2_LDS);
    k1_kernel<<<256, 512, K1_LDS, stream>>>((const char*)d_in[0], (const unsigned short*)d_in[1], (const unsigned short*)d_in[2],
                                            (const char*)d_in[4], (char*)d_ws);
    k2_kernel<<<256, 512, K2_LDS, stream>>>((const char*)d_in[0], (const char*)d_in[5], (char*)d_ws, (char*)d_out);
}
```

```cpp
#include <hip/hip_runtime.h>
#include <stdint.h>
#pragma clang diagnostic ignored "-Wint-to-pointer-cast"

typedef short s16x4 __attribute__((ext_vector_type(4)));
typedef __bf16 bf16x8 __attribute__((ext_vector_type(8)));
typedef __bf16 bf16x2 __attribute__((ext_vector_type(2)));
typedef float f32x4 __attribute__((ext_vector_type(4)));
typedef float f32x2 __attribute__((ext_vector_type(2)));
typedef unsigned u32x4 __attribute__((ext_vector_type(4)));
typedef unsigned u32x2 __attribute__((ext_vector_type(2)));
typedef unsigned long long u64;
#define LAS __attribute__((address_space(3)))
#define GAS __attribute__((address_space(1)))
#define DEVI __device__ __forceinline__

constexpr size_t WS_H = 0;
constexpr size_t WS_X1T = 8u << 20;
constexpr size_t WS_WT = 12u << 20;
constexpr size_t WS_ZT = 16u << 20;
constexpr size_t WS_FLAG = 20u << 20;
constexpr size_t WS_TS = 24u << 20;
constexpr unsigned FL_H = 4096, FL_A = 8192, FL_Z = 8192 + 16384;

constexpr unsigned L_W1B = 65536, L_F0 = 131072, L_F1 = 32768;
constexpr unsigned L_CS = 161792, L_ST = L_CS + 512, L_SY = L_ST + 1024, L_END = L_SY + 64;
constexpr unsigned L_Z = 81920, L_BP = 131072, L_ZS = L_BP + 18432, L_WS = 147456;
constexpr unsigned SY_TEAM = L_SY, SY_ZARR = L_SY + 4, SY_ZOK = L_SY + 8, SY_AARR = L_SY + 12, SY_AOK = L_SY + 16, SY_HARR = L_SY + 20,
                   SY_BOK = L_SY + 24, SY_CS2 = L_SY + 28, SY_H47 = L_SY + 32, SY_W2OK = L_SY + 36;
constexpr unsigned SPIN_CAP = 1u << 16;

#define STAMP2(id) do { } while (0)
#define STAMP(id) do { } while (0)

DEVI unsigned pk2(float a, float b) {
    f32x2 f = {a, b};
    return __builtin_bit_cast(unsigned, __builtin_convertvector(f, bf16x2));
}
DEVI float bf_round(float a) { return __builtin_bit_cast(float, pk2(a, a) << 16); }
DEVI float bf_lo(unsigned u) { return __builtin_bit_cast(float, u << 16); }
DEVI float bf_hi(unsigned u) { return __builtin_bit_cast(float, u & 0xffff0000u); }
DEVI f32x4 mfma(u32x4 a, u32x4 b, f32x4 c) {
    return __builtin_amdgcn_mfma_f32_16x16x32_bf16(__builtin_bit_cast(bf16x8, a), __builtin_bit_cast(bf16x8, b), c, 0, 0, 0);
}
DEVI u32x2 trread(unsigned addr) {
    s16x4 v = __builtin_amdgcn_ds_read_tr16_b64_v4i16((LAS s16x4*)addr);
    return __builtin_bit_cast(u32x2, v);
}
DEVI u32x4 trread2(unsigned a0, unsigned a1) {
    u32x2 lo = trread(a0), hi = trread(a1);
    u32x4 r = {lo[0], lo[1], hi[0], hi[1]};
    return r;
}
DEVI u32x4 lds_rd16(unsigned addr) { return *(LAS u32x4*)addr; }
DEVI void lds_wr16(unsigned addr, u32x4 v) { *(LAS u32x4*)addr = v; }
DEVI void lds_wr8(unsigned addr, u32x2 v) { *(LAS u32x2*)addr = v; }
DEVI float cosr(float rev) { return __builtin_amdgcn_cosf(rev); }
DEVI float sinr(float rev) { return __builtin_amdgcn_sinf(rev); }
DEVI void barrier() { asm volatile("s_barrier" ::: "memory"); }
#define WAIT_VM(n) asm volatile("s_waitcnt vmcnt(%0)" ::"n"(n) : "memory")
#define WAIT_LGKM0() asm volatile("s_waitcnt lgkmcnt(0)" ::: "memory")
#define PIN4(a) asm volatile("" : "+v"(a))
DEVI __amdgpu_buffer_rsrc_t rsrc(char* base, unsigned bytes) { return __builtin_amdgcn_make_buffer_rsrc(base, 0, bytes, 0x00020000); }
DEVI void st_sc1(u32x4 v, __amdgpu_buffer_rsrc_t r, unsigned off) { __builtin_amdgcn_raw_buffer_store_b128(v, r, off, 0, 16); }
DEVI u32x4 ld_sc1(__amdgpu_buffer_rsrc_t r, unsigned off) { return __builtin_amdgcn_raw_buffer_load_b128(r, off, 0, 16); }

DEVI void dma16(unsigned voff, const void* sbase, unsigned dst) {
    unsigned keep;
    asm volatile("s_mov_b32 %0, m0\n\ts_mov_b32 m0, %3\n\ts_nop 0\n\tglobal_load_lds_dwordx4 %1, %2\n\ts_mov_b32 m0, %0"
                 : "=&s"(keep) : "v"(voff), "s"(sbase), "s"(dst) : "memory");
}
template <unsigned IMM>
DEVI void dmaq(unsigned voff, const void* sbase, unsigned sdst) {
    asm volatile("s_add_u32 m0, %2, %3\n\ts_nop 0\n\tglobal_load_lds_dwordx4 %0, %1" :: "v"(voff), "s"(sbase), "s"(sdst), "n"(IMM) : "memory", "scc");
}
DEVI unsigned lds_ld_relaxed(unsigned addr) { return __hip_atomic_load((LAS unsigned*)addr, __ATOMIC_RELAXED, __HIP_MEMORY_SCOPE_WORKGROUP); }
DEVI unsigned lds_arrive(unsigned cnt, int lane) {
    unsigned old = 0;
    if (lane == 0) old = __hip_atomic_fetch_add((LAS unsigned*)cnt, 1u, __ATOMIC_RELAXED, __HIP_MEMORY_SCOPE_WORKGROUP);
    return __builtin_amdgcn_readfirstlane(old);
}
DEVI void lds_wait_ge(unsigned cnt, unsigned target) {
#pragma unroll 1
    for (unsigned s = 0; s < SPIN_CAP * 16; ++s) {
        asm volatile("" ::: "memory");
        if ((unsigned)__builtin_amdgcn_readfirstlane(lds_ld_relaxed(cnt)) >= target) break;
        __builtin_amdgcn_s_sleep(1);
    }
    asm volatile("" ::: "memory");
}
DEVI void team_sync(unsigned cnt, unsigned target, int lane) {
    WAIT_LGKM0();
    if (lane == 0) __hip_atomic_fetch_add((LAS unsigned*)cnt, 1u, __ATOMIC_RELAXED, __HIP_MEMORY_SCOPE_WORKGROUP);
    lds_wait_ge(cnt, target);
}
DEVI u64 epoch_request(const char* ws) { return __hip_atomic_load((const GAS u64*)(ws + WS_FLAG), __ATOMIC_RELAXED, __HIP_MEMORY_SCOPE_AGENT); }
DEVI u64 epoch_finish(u64 e) {
    const u64 v = ((u64)(unsigned)__builtin_amdgcn_readfirstlane((unsigned)(e >> 32)) << 32) | (unsigned)__builtin_amdgcn_readfirstlane((unsigned)e);
    return v + 1;
}
DEVI void post_replicas(char* ws, unsigned off, int b, int lane, u64 epoch) {
    if (lane < 8) __hip_atomic_store((GAS u64*)(ws + WS_FLAG + off + lane * 2048 + b * 8), epoch, __ATOMIC_RELAXED, __HIP_MEMORY_SCOPE_AGENT);
}
DEVI void poll256(const char* ws, unsigned off, int b, int lane, u64 epoch) {
    const char* fb = ws + WS_FLAG + off + (b & 7) * 2048;
    const unsigned foff = lane * 16, lo = (unsigned)epoch, hi = (unsigned)(epoch >> 32);
#pragma unroll 1
    for (unsigned s = 0; s < SPIN_CAP; ++s) {
        u32x4 f0, f1;
        asm volatile("global_load_dwordx4 %0, %2, %3 sc1\n\tglobal_load_dwordx4 %1, %2, %3 offset:1024 sc1\n\ts_waitcnt vmcnt(0)"
                     : "=&v"(f0), "=&v"(f1) : "v"(foff), "s"(fb) : "memory");
        const unsigned d = ((f0[0] ^ lo) | (f0[2] ^ lo) | (f1[0] ^ lo) | (f1[2] ^ lo)) | ((f0[1] ^ hi) | (f0[3] ^ hi) | (f1[1] ^ hi) | (f1[3] ^ hi));
        if (__all(d == 0u)) break;
        __builtin_amdgcn_s_sleep(2);
    }
    asm volatile("" ::: "memory");
}

DEVI void h_epilogue(const f32x4& a0, const f32x4& a1, unsigned csa, float mb, float rr, __amdgpu_buffer_rsrc_t rh, unsigned off) {
    const f32x4 c0 = *(LAS f32x4*)(csa), c1 = *(LAS f32x4*)(csa + 64);
    float v[8];
#pragma unroll
    for (int r = 0; r < 4; ++r) {
        v[r] = fmaxf(rr * (a0[r] - mb * c0[r]), 0.0f);
        v[4 + r] = fmaxf(rr * (a1[r] - mb * c1[r]), 0.0f);
    }
    const u32x4 o = {pk2(v[0], v[1]), pk2(v[2], v[3]), pk2(v[4], v[5]), pk2(v[6], v[7])};
    st_sc1(o, rh, off);
}

constexpr int Y_SLOTS = 5;
DEVI void w2_issue(const char* wb, unsigned voffw, int w, int i) {
#pragma unroll
    for (int pc = 0; pc < 4; ++pc) dma16(voffw, wb + (size_t)(i * 128 + pc * 8) * 1024, ((i % Y_SLOTS) * 4 + w) * 4096 + pc * 1024);
}

template <int I>
DEVI void w2_step(const char* wb, unsigned voffw, unsigned sd) {
    const char* sb = wb + (size_t)I * 128 * 1024;
    dmaq<(I % Y_SLOTS) * 16384>(voffw, sb, sd);
    dmaq<(I % Y_SLOTS) * 16384 + 1024>(voffw + 8192, sb, sd);
    dmaq<(I % Y_SLOTS) * 16384 + 2048>(voffw + 16384, sb, sd);
    dmaq<(I % Y_SLOTS) * 16384 + 3072>(voffw + 24576, sb, sd);
}
constexpr int X_AHEAD = 6;
template <int NF, int NG>
DEVI void m_first(const char* x, char* ws, u32x4 (&xr)[16][2], float (&mb)[2], float (&rr)[2], __amdgpu_buffer_rsrc_t rh, unsigned hoff,
                  int P, int w, int lane, int fg0, int rot) {
    const int n = lane & 15, g = lane >> 4, q = n >> 2, p = n & 3;
    const int t0 = 2 * P + (w >> 1);
    const unsigned tl0 = 8 * (n >> 2) + (n & 3);
    const unsigned xoff = (32u * (32 * (w & 1) + tl0) + t0) * 1024u + g * 16;
    const GAS char* xb = (const GAS char*)x;
    auto ldx = [&](int ks) {
        const GAS char* xk = xb + ((((ks >> 1) + rot) & 7) << 7);
        xr[ks][0] = *(const GAS u32x4*)(xk + (xoff + (ks & 1) * 64));
        xr[ks][1] = *(const GAS u32x4*)(xk + (xoff + 131072u + (ks & 1) * 64));
    };
#pragma unroll
    for (int ks = 0; ks < X_AHEAD; ++ks) ldx(ks);
    asm volatile("" ::: "memory");
    f32x4 acc[2][NG], af[2][NF], aS[2], aQ[2];
#pragma unroll
    for (int i = 0; i < NG; ++i) { acc[0][i] = f32x4{0, 0, 0, 0}; acc[1][i] = f32x4{0, 0, 0, 0}; }
#pragma unroll
    for (int f = 0; f < NF; ++f) { af[0][f] = f32x4{0, 0, 0, 0}; af[1][f] = f32x4{0, 0, 0, 0}; }
    aS[0] = aS[1] = aQ[0] = aQ[1] = f32x4{0, 0, 0, 0};
    const u32x4 ones = {0x3f803f80u, 0x3f803f80u, 0x3f803f80u, 0x3f803f80u};
    {
        constexpr unsigned PITCH = (NG == 4) ? 128 : 64;
        const unsigned swz = (NG == 4) ? ((q >> 1) | ((g & 1) << 1)) : (g & 1);
        const unsigned abase = (8 * g + q) * PITCH + swz * 32 + p * 8;
        u32x4 A[NG], F[NF];
        auto rdA = [&](int ks, int db) -> u32x4 { const unsigned a = (abase ^ (db << 5)) + ks * 32 * PITCH; return trread2(a, a + 4 * PITCH); };
        auto rdF = [&](int ks, int f) -> u32x4 { return lds_rd16(((f == 0) ? L_F0 : L_F1 + (f - 1) * 16384) + ks * 1024 + lane * 16); };
        STAMP(1);
        WAIT_LGKM0();
        STAMP2(0);
        barrier();
        STAMP2(8);
        STAMP(2);
#pragma unroll
        for (int db = 0; db < NG; ++db) A[db] = rdA(0, db);
#pragma unroll
        for (int f = 0; f < NF; ++f) F[f] = rdF(0, f);
#pragma unroll
        for (int ks = 0; ks < 16; ++ks) {
            if (ks + X_AHEAD < 16) ldx(ks + X_AHEAD);
            if ((ks & 1) && ks < 15) { STAMP2((ks + 1) / 2); barrier(); STAMP2(8 + (ks + 1) / 2); }
#pragma unroll
            for (int db = 0; db < 4; ++db) {
                if (db < NG) {
                    acc[0][db] = mfma(A[db], xr[ks][0], acc[0][db]);
                    acc[1][db] = mfma(A[db], xr[ks][1], acc[1][db]);
                    if (ks < 15) A[db] = rdA(ks + 1, db);
                }
                if (db < 2) aS[db] = mfma(ones, xr[ks][db], aS[db]);
                else aQ[db - 2] = mfma(xr[ks][db - 2], xr[ks][db - 2], aQ[db - 2]);
                __builtin_amdgcn_sched_barrier(0);
            }
#pragma unroll
            for (int f = 0; f < NF; ++f) {
                af[0][f] = mfma(xr[ks][0], F[f], af[0][f]);
                af[1][f] = mfma(xr[ks][1], F[f], af[1][f]);
                if (ks < 15) F[f] = rdF(ks + 1, f);
                __builtin_amdgcn_sched_barrier(0);
            }
        }
    }
    STAMP(3);
#pragma unroll
    for (int tt = 0; tt < 2; ++tt) {
        const float sq = (p & 2) ? ((p & 1) ? aQ[tt][3] : aQ[tt][2]) : ((p & 1) ? aQ[tt][1] : aQ[tt][0]);
        const float mean = aS[tt][0] * (1.0f / 512.0f);
        const float var = fmaxf(sq * (1.0f / 512.0f) - mean * mean, 0.0f);
        const float m = bf_round(mean);
        const float den = bf_round(__builtin_sqrtf(bf_round(bf_round(var) + 1e-5f)));
        const float r = 1.0f / den;
        if (g == q) lds_wr8(L_ST + (w * 32 + tt * 16 + n) * 8, u32x2{__builtin_bit_cast(unsigned, m), __builtin_bit_cast(unsigned, r)});
    }
    WAIT_LGKM0();
    {
        f32x4 st[2][2];
#pragma unroll
        for (int tt = 0; tt < 2; ++tt) {
            st[tt][0] = *(LAS f32x4*)(L_ST + (w * 32 + tt * 16 + 4 * g) * 8);
            st[tt][1] = *(LAS f32x4*)(L_ST + (w * 32 + tt * 16 + 4 * g) * 8 + 16);
            const f32x2 s = *(LAS f32x2*)(L_ST + (w * 32 + tt * 16 + n) * 8);
            mb[tt] = s[0]; rr[tt] = s[1];
        }
        const __amdgpu_buffer_rsrc_t rx = rsrc(ws + WS_X1T, 512 * 4096);
#pragma unroll
        for (int f = 0; f < NF; ++f) {
            const int c = 16 * (fg0 + f) + n;
            const float cs = (c == 0) ? 512.0f : 0.0f;
            float v[8];
#pragma unroll
            for (int tt = 0; tt < 2; ++tt)
#pragma unroll
                for (int r = 0; r < 4; ++r) v[4 * tt + r] = st[tt][r >> 1][2 * (r & 1) + 1] * (af[tt][f][r] - st[tt][r >> 1][2 * (r & 1)] * cs);
            const u32x4 o = {pk2(v[0], v[1]), pk2(v[2], v[3]), pk2(v[4], v[5]), pk2(v[6], v[7])};
            st_sc1(o, rx, (c >> 1) * 8192 + (4 * P + w) * 128 + (c & 1) * 64 + g * 16);
        }
    }
    STAMP(5);
    barrier();
    STAMP(4);
#pragma unroll
    for (int i2 = 0; i2 < NG / 2; ++i2)
#pragma unroll
        for (int tt = 0; tt < 2; ++tt)
            h_epilogue(acc[tt][2 * i2], acc[tt][2 * i2 + 1], L_CS + (32 * i2 + 4 * g) * 4, mb[tt], rr[tt], rh, hoff + (i2 * 4 + tt) * 1024);
}

DEVI void m_phase_a(const char* x, char* ws, const char* wb, unsigned voffw, int b, int P, bool typeA, int w, int lane, int ii0, int fg0, int hj, int rot, u64& epoch) {
    const int n = lane & 15, g = lane >> 4, q = n >> 2, p = n & 3;
    const u64 ereq = epoch_request(ws);
    u32x4 xr[16][2];
    float mb[2], rr[2];
    const __amdgpu_buffer_rsrc_t rh = rsrc(ws + WS_H, 8u << 20);
    const unsigned hoff = (((2 * P + (w >> 1)) * 56 + ii0) * 4 + 2 * (w & 1)) * 1024 + lane * 16;
    if (typeA) m_first<1, 4>(x, ws, xr, mb, rr, rh, hoff, P, w, lane, fg0, rot);
    else m_first<3, 2>(x, ws, xr, mb, rr, rh, hoff, P, w, lane, fg0, rot);
    if (typeA) WAIT_VM(4); else WAIT_VM(2);
    epoch = epoch_finish(ereq);
    if (lds_arrive(SY_AARR, lane) == 7) post_replicas(ws, FL_A, b, lane, epoch);
    lds_wait_ge(SY_BOK, 4);
    STAMP(6);
    f32x4 a2[2][4];
#pragma unroll
    for (int i = 0; i < 4; ++i) { a2[0][i] = f32x4{0, 0, 0, 0}; a2[1][i] = f32x4{0, 0, 0, 0}; }
    {
        const unsigned swz = (q >> 1) | ((g & 1) << 1);
        const unsigned abase = L_W1B + (8 * g + q) * 128 + swz * 32 + p * 8;
        u32x4 A[4];
        auto rdA = [&](int ks, int db) -> u32x4 { const unsigned a = (abase ^ (db << 5)) + ks * 4096; return trread2(a, a + 512); };
#pragma unroll
        for (int db = 0; db < 4; ++db) A[db] = rdA(0, db);
#pragma unroll
        for (int ks = 0; ks < 16; ++ks) {
#pragma unroll
            for (int db = 0; db < 4; ++db) {
                a2[0][db] = mfma(A[db], xr[ks][0], a2[0][db]);
                a2[1][db] = mfma(A[db], xr[ks][1], a2[1][db]);
                if (ks < 15) A[db] = rdA(ks + 1, db);
                __builtin_amdgcn_sched_barrier(0);
            }
        }
    }
    WAIT_LGKM0();
    (void)lds_arrive(SY_H47, lane);
    STAMP(7);
    lds_wait_ge(SY_CS2, 4);
    const unsigned hoff2 = hoff + (typeA ? 8192 : 4096);
#pragma unroll
    for (int i2 = 0; i2 < 2; ++i2)
#pragma unroll
        for (int tt = 0; tt < 2; ++tt)
            h_epilogue(a2[tt][2 * i2], a2[tt][2 * i2 + 1], L_CS + (64 + 32 * i2 + 4 * g) * 4, mb[tt], rr[tt], rh, hoff2 + (i2 * 4 + tt) * 1024);
    WAIT_VM(0);
    if (lds_arrive(SY_HARR, lane) == 3 && lane == 0)
        __hip_atomic_store((GAS u64*)(ws + WS_FLAG + FL_H + P * 128 + hj * 8), epoch, __ATOMIC_RELAXED, __HIP_MEMORY_SCOPE_AGENT);
    STAMP(8);
}

#define GLOAD16(dst, voff, sbase, imm) asm volatile("global_load_dwordx4 %0, %1, %2 offset:%3 sc1" : "=v"(dst) : "v"(voff), "s"(sbase), "n"(imm) : "memory")
constexpr int y_after(int i) {
    int cnt = 0;
    if (i <= 3) cnt += 4 * (3 - i);
    const int j0 = (i <= 3) ? 0 : (i - 3);
    for (int j = j0; j < i; ++j) { if (j + Y_SLOTS < 14) cnt += 4; if (j + 4 < 14) cnt += 4; }
    return cnt;
}

DEVI void m_team(const char* x, const char* __restrict__ w2, char* ws, int b, int P, bool typeA, int ii0, int fg0, int hj,
                 int tile64, int d0, int rot, int w, int lane, unsigned ooff, u32x4& xv) {
    const int n = lane & 15, g = lane >> 4, q = n >> 2, p = n & 3;
    const int r8 = lane >> 3, p16 = lane & 7;
    const unsigned voffw = r8 * 1024 + ((p16 ^ (2 * ((r8 >> 1) & 3))) * 16);
    const char* wb = w2 + d0 * 2 + (size_t)w * 32 * 1024;
    u64 epoch;
    m_phase_a(x, ws, wb, voffw, b, P, typeA, w, lane, ii0, fg0, hj, rot, epoch);

    lds_wait_ge(SY_H47, 4);
    w2_issue(wb, voffw, w, 4);
    lds_wait_ge(SY_W2OK, 4);
    xv = *(const GAS u32x4*)((const GAS char*)x + ooff);
    {
        const char* fb = ws + WS_FLAG + FL_H + (tile64 >> 1) * 128;
        const unsigned foff = (lane & 15) * 8, lo = (unsigned)epoch, hi = (unsigned)(epoch >> 32);
#pragma unroll 1
        for (unsigned s = 0; s < SPIN_CAP; ++s) {
            u32x2 f;
            asm volatile("global_load_dwordx2 %0, %1, %2 sc1\n\ts_waitcnt vmcnt(0)" : "=v"(f) : "v"(foff), "s"(fb) : "memory");
            if (__all(((f[0] ^ lo) | (f[1] ^ hi)) == 0u)) break;
            __builtin_amdgcn_s_sleep(2);
        }
        asm volatile("" ::: "memory");
    }
    STAMP(9);
    const char* hb = ws + WS_H + (size_t)(tile64 * 56 + w) * 4096;
    const unsigned voffh = lane * 16;
    u32x4 h[4][4];
    auto issue_h = [&](int i) {
        const char* hp = hb + (size_t)i * 16384;
        GLOAD16(h[i & 3][0], voffh, hp, 0); GLOAD16(h[i & 3][1], voffh, hp, 1024);
        GLOAD16(h[i & 3][2], voffh, hp, 2048); GLOAD16(h[i & 3][3], voffh, hp, 3072);
    };
#pragma unroll
    for (int i = 0; i < 4; ++i) issue_h(i);
    f32x4 ay[4][4];
#pragma unroll
    for (int a = 0; a < 4; ++a)
#pragma unroll
        for (int t = 0; t < 4; ++t) ay[a][t] = f32x4{0, 0, 0, 0};
    const unsigned abase = w * 4096 + (4 * g + q) * 128 + (((2 * g + (q >> 1)) & 3) * 32) + p * 8;
    u32x4 A[2][4];
    auto rdA = [&](int i, u32x4 (&dst)[4]) {
#pragma unroll
        for (int db = 0; db < 4; ++db) { const unsigned a = (abase ^ (db << 5)) + (i % Y_SLOTS) * 16384; dst[db] = trread2(a, a + 2048); }
    };
#define T1_WAIT(N, slot) asm volatile("s_waitcnt vmcnt(%4)" : "+v"(h[slot][0]), "+v"(h[slot][1]), "+v"(h[slot][2]), "+v"(h[slot][3]) : "n"(N) : "memory")
    rdA(0, A[0]);
#pragma unroll
    for (int i = 0; i < 14; ++i) {
        T1_WAIT(y_after(i), i & 3);
        if (i == 0) STAMP(10);
        if (i + 1 < 14) rdA(i + 1, A[(i + 1) & 1]);
#pragma unroll
        for (int db = 0; db < 4; ++db)
#pragma unroll
            for (int t = 0; t < 4; ++t) ay[db][t] = mfma(A[i & 1][db], h[i & 3][t], ay[db][t]);
        __builtin_amdgcn_sched_barrier(0);
        if (i + Y_SLOTS < 14) w2_issue(wb, voffw, w, i + Y_SLOTS);
        if (i + 4 < 14) issue_h(i + 4);
    }
    STAMP(11);
#pragma unroll
    for (int t = 0; t < 4; ++t)
#pragma unroll
        for (int db = 0; db < 4; ++db) *(LAS f32x4*)((t * 4 + w) * 4096 + db * 1024 + lane * 16) = ay[db][t];
}

template <bool HAS_I>
DEVI void p2_unit(char* ws, const u32x4 (&Ur)[2][2], const u32x4 (&Ui)[2][2], const u32x4 (&Wf)[2], int zrow, int v, int lane, unsigned& sync_n,
                  const u32x4 (&C64)[2], const u32x4 (&S64)[2], const u32x4 (&N64)[2],
                  const u32x4 (&C32)[2], const u32x4 (&S32)[2], const u32x4 (&N32)[2],
                  const float (&tc)[8], const float (&tsn)[8]) {
    const int n = lane & 15, g = lane >> 4, q = n >> 2, p = n & 3;
    float br[8], bi[8];
#pragma unroll
    for (int mt = 0; mt < 2; ++mt) {
        f32x4 dr = {0, 0, 0, 0}, di = {0, 0, 0, 0};
#pragma unroll
        for (int ks = 0; ks < 2; ++ks) {
            dr = mfma(Ur[mt][ks], C64[ks], dr);
            di = mfma(Ur[mt][ks], N64[ks], di);
            if (HAS_I) { dr = mfma(Ui[mt][ks], S64[ks], dr); di = mfma(Ui[mt][ks], C64[ks], di); }
        }
#pragma unroll
        for (int r = 0; r < 4; ++r) {
            const float c = tc[4 * mt + r], s = tsn[4 * mt + r];
            br[4 * mt + r] = dr[r] * c + di[r] * s;
            bi[4 * mt + r] = di[r] * c - dr[r] * s;
        }
    }
    u32x4 Br = {pk2(br[0], br[1]), pk2(br[2], br[3]), pk2(br[4], br[5]), pk2(br[6], br[7])};
    u32x4 Bi = {pk2(bi[0], bi[1]), pk2(bi[2], bi[3]), pk2(bi[4], bi[5]), pk2(bi[6], bi[7])};
    float yr[8], yi[8];
#pragma unroll
    for (int t = 0; t < 2; ++t) {
        f32x4 dr = {0, 0, 0, 0}, di = {0, 0, 0, 0};
        dr = mfma(C32[t], Br, dr); dr = mfma(S32[t], Bi, dr);
        di = mfma(C32[t], Bi, di); di = mfma(N32[t], Br, di);
#pragma unroll
        for (int r = 0; r < 4; ++r) {
            const float wre = bf_lo(Wf[t][r]), wim = bf_hi(Wf[t][r]);
            yr[4 * t + r] = dr[r] * wre - di[r] * wim;
            yi[4 * t + r] = dr[r] * wim + di[r] * wre;
        }
    }
    u32x4 Yr = {pk2(yr[0], yr[1]), pk2(yr[2], yr[3]), pk2(yr[4], yr[5]), pk2(yr[6], yr[7])};
    u32x4 Yi = {pk2(yi[0], yi[1]), pk2(yi[2], yi[3]), pk2(yi[4], yi[5]), pk2(yi[6], yi[7])};
#pragma unroll
    for (int mt = 0; mt < 2; ++mt) {
        f32x4 dr = {0, 0, 0, 0}, di = {0, 0, 0, 0};
        dr = mfma(C32[mt], Yr, dr); dr = mfma(N32[mt], Yi, dr);
        di = mfma(C32[mt], Yi, di); di = mfma(S32[mt], Yr, di);
        float pr[4], pi[4];
#pragma unroll
        for (int r = 0; r < 4; ++r) {
            const float c = tc[4 * mt + r] * (1.0f / 2048.0f), s = tsn[4 * mt + r] * (1.0f / 2048.0f);
            pr[r] = dr[r] * c - di[r] * s;
            pi[r] = di[r] * c + dr[r] * s;
        }
        const unsigned a = L_BP + (16 * v + n) * 72 + (16 * mt + 4 * g) * 2;
        lds_wr8(a, u32x2{pk2(pr[0], pr[1]), pk2(pr[2], pr[3])});
        lds_wr8(a + 64 * 72, u32x2{pk2(pi[0], pi[1]), pk2(pi[2], pi[3])});
    }
    sync_n += 4; team_sync(SY_TEAM, sync_n, lane);
#pragma unroll
    for (int nt = 0; nt < 2; ++nt) {
        f32x4 dr = {0, 0, 0, 0}, di = {0, 0, 0, 0};
#pragma unroll
        for (int ks = 0; ks < 2; ++ks) {
            const unsigned a = L_BP + (32 * ks + 8 * g + q) * 72 + nt * 32 + p * 8;
            const u32x4 pr = trread2(a, a + 4 * 72), pi = trread2(a + 64 * 72, a + 64 * 72 + 4 * 72);
            dr = mfma(C64[ks], pr, dr); dr = mfma(N64[ks], pi, dr);
            if (HAS_I) { di = mfma(C64[ks], pi, di); di = mfma(S64[ks], pr, di); }
        }
        const unsigned a = L_ZS + (16 * nt + n) * 144 + (16 * v + 4 * g) * 2;
        lds_wr8(a, u32x2{pk2(dr[0], dr[1]), pk2(dr[2], dr[3])});
        if (HAS_I) lds_wr8(a + 32 * 144, u32x2{pk2(di[0], di[1]), pk2(di[2], di[3])});
    }
    sync_n += 4; team_sync(SY_TEAM, sync_n, lane);
    {
        const __amdgpu_buffer_rsrc_t rs = rsrc(ws + WS_ZT, 512 * 4096);
#pragma unroll
        for (int it = 0; it < (HAS_I ? 2 : 1); ++it) {
            const int row = 8 * v + (lane >> 3), ch = lane & 7;
            const u32x4 o = lds_rd16(L_ZS + (it * 32 + row) * 144 + ch * 16);
            st_sc1(o, rs, (zrow + it) * 4096 + row * 128 + ch * 16);
        }
    }
}

#define GLOAD_U16(dst, voff, sbase, imm) asm volatile("global_load_ushort %0, %1, %2 offset:%3" : "=v"(dst) : "v"(voff), "s"(sbase), "n"(imm) : "memory")
template <int S> struct IC { static constexpr int v = S; };
DEVI void f_gen(int fg, unsigned foff, int rot, int u, int lane) {
    const int n = lane & 15, g = lane >> 4;
    const int c = 16 * fg + n;
    const int k2 = (c == 1) ? 256 : (c >> 1), part = (c == 1) ? 0 : (c & 1);
    const float st = (float)k2 * (1.0f / 512.0f), ph = (float)part * 0.25f;
    const unsigned sg = (k2 & 1) ? 0x80008000u : 0u;
    const float d0 = (float)(64 * u + 8 * g + 64 * rot);
#pragma unroll
    for (int e = 0; e < 2; ++e) {
        const float x0 = __builtin_amdgcn_fractf(st * (d0 + 32.0f * e) + ph);
        float v[8];
#pragma unroll
        for (int j = 0; j < 8; ++j) v[j] = cosr(x0 + (float)j * st);
        const u32x4 o = {pk2(v[0], v[1]), pk2(v[2], v[3]), pk2(v[4], v[5]), pk2(v[6], v[7])};
        const u32x4 o2 = {o[0] ^ sg, o[1] ^ sg, o[2] ^ sg, o[3] ^ sg};
        lds_wr16(foff + (2 * u + e) * 1024 + lane * 16, o);
        lds_wr16(foff + (2 * u + e + 8) * 1024 + lane * 16, o2);
    }
}
DEVI void s_team(const char* x, const char* __restrict__ w1, const char* __restrict__ w2, const unsigned short* wr, const unsigned short* wi, char* ws, int b,
                 bool typeA, int col0, int fg0, int tile64, int d0, int rot, int u, int lane, unsigned ooff, u32x4& xv) {
    const int n = lane & 15, g = lane >> 4, q = n >> 2, p = n & 3;
    const u64 ereq = epoch_request(ws);
    const char* wb = w1 + col0 * 2;
    const int r8 = lane >> 3, p16 = lane & 7;
    const unsigned v128 = r8 * 3584 + (p16 & 1) * 16;
    unsigned va[2];
    if (typeA) {
#pragma unroll
        for (int j = 0; j < 2; ++j) va[j] = v128 + (((p16 >> 1) ^ (((r8 >> 1) & 1) | (j << 1))) * 32) + (2 * u + j) * 8 * 3584;
    } else {
        const int r16 = lane >> 2, c4 = lane & 3;
        va[0] = va[1] = r16 * 3584 + (((c4 >> 1) ^ (r16 >> 3)) * 32) + (c4 & 1) * 16 + u * 16 * 3584;
    }
    const unsigned sdA = typeA ? u * 2048 : u * 1024;
    auto stage_a = [&](auto S) {
        constexpr int s = decltype(S)::v;
        const char* sb = wb + (size_t)(((s + rot) & 7) * (64 * 3584));
        if (typeA) { dmaq<s * 8192>(va[0], sb, sdA); dmaq<s * 8192 + 1024>(va[1], sb, sdA); }
        else dmaq<s * 4096>(va[0], sb, sdA);
    };
    stage_a(IC<0>{}); stage_a(IC<1>{});
    const int rs = b & 63, cq = b >> 6;
    const int jv = rs & 3, ja = (rs >> 2) & 1, jt = (rs >> 3) & 1, jg = rs >> 4;
    unsigned wv[2][4][2], wsp;
    {
        const unsigned k0 = 16 * jv + 8 * ja + 2 * u + 64 * (16 * jt + 4 * jg);
        const unsigned off0 = (k0 * 257 + 64 * cq + lane) * 2;
#pragma unroll
        for (int r = 0; r < 4; ++r) {
            const unsigned off = off0 + r * (64 * 514);
            GLOAD_U16(wv[0][r][0], off, wr, 0); GLOAD_U16(wv[0][r][1], off, wi, 0);
            GLOAD_U16(wv[1][r][0], off, wr, 514); GLOAD_U16(wv[1][r][1], off, wi, 514);
        }
        const int unit = 2 * b + ((lane >> 3) & 1), reg = (lane >> 1) & 3;
        const unsigned k1 = 16 * (unit >> 7) + (unit & 15) + 64 * (16 * ((unit >> 6) & 1) + 4 * ((unit >> 4) & 3) + reg);
        const unsigned long long a = (unsigned long long)((lane & 1) ? wi : wr) + (k1 * 257 + 256) * 2;
        asm volatile("global_load_ushort %0, %1, off" : "=v"(wsp) : "v"(a) : "memory");
    }
    stage_a(IC<2>{});
    STAMP(1);
    f_gen(fg0, L_F0, rot, u, lane);
    if (!typeA) { f_gen(fg0 + 1, L_F1, rot, u, lane); f_gen(fg0 + 2, L_F1 + 16384, rot, u, lane); }
    STAMP(2);
    const char* wb2 = wb + (typeA ? 128 : 64);
    const unsigned vbb = v128 + (((p16 >> 1) ^ (((r8 >> 1) & 1) | ((u & 1) << 1))) * 32) + u * 8 * 3584;
    const unsigned sdB = L_W1B + u * 1024;
    auto stage_b = [&](auto K) {
        constexpr int k = decltype(K)::v;
        dmaq<k * 4096>(vbb, wb2 + (size_t)(((k + 2 * rot) & 15) * (32 * 3584)), sdB);
    };
    const u32x4 ones = {0x3f803f80u, 0x3f803f80u, 0x3f803f80u, 0x3f803f80u};
    {
        const unsigned swz = (q >> 1) | ((g & 1) << 1);
        const unsigned abase = typeA ? (((8 * g + q) * 128 + swz * 32 + p * 8) ^ (u << 5)) : ((8 * g + q) * 64 + (((u ^ g) & 1) * 32) + p * 8);
        const unsigned kpitch = typeA ? 4096 : 2048, hpitch = typeA ? 512 : 256;
        f32x4 cs = {0, 0, 0, 0};
        auto stage = [&](auto S) {
            constexpr int s = decltype(S)::v;
            STAMP2(s);
            if (typeA) {
                if (s < 2) WAIT_VM(21); else if (s < 5) WAIT_VM(4); else if (s == 5) WAIT_VM(6); else if (s == 6) WAIT_VM(12); else WAIT_VM(18);
            } else {
                if (s < 2) WAIT_VM(19); else if (s < 5) WAIT_VM(2); else if (s == 5) WAIT_VM(4); else if (s == 6) WAIT_VM(11); else WAIT_VM(18);
            }
            WAIT_LGKM0();
            STAMP2(8 + s);
            barrier();
            if (s == 0) STAMP(3);
            const unsigned a0 = abase + (2 * s) * kpitch, a1 = a0 + kpitch;
            const u32x4 c0 = trread2(a0, a0 + hpitch), c1 = trread2(a1, a1 + hpitch);
            if constexpr (s + 3 < 8) stage_a(IC<s + 3>{});
            if constexpr (s == 2) {
                asm volatile("" : "+v"(wv[0][0][0]), "+v"(wv[0][0][1]), "+v"(wv[0][1][0]), "+v"(wv[0][1][1]), "+v"(wv[0][2][0]), "+v"(wv[0][2][1]), "+v"(wv[0][3][0]), "+v"(wv[0][3][1]),
                                  "+v"(wv[1][0][0]), "+v"(wv[1][0][1]), "+v"(wv[1][1][0]), "+v"(wv[1][1][1]), "+v"(wv[1][2][0]), "+v"(wv[1][2][1]), "+v"(wv[1][3][0]), "+v"(wv[1][3][1]), "+v"(wsp));
#pragma unroll
                for (int e = 0; e < 2; ++e) {
                    u32x4 o;
#pragma unroll
                    for (int r = 0; r < 4; ++r) o[r] = wv[e][r][0] | (wv[e][r][1] << 16);
                    lds_wr16(L_WS + lane * 144 + (2 * u + e) * 16, o);
                }
            }
            if constexpr (s == 4) {
                const __amdgpu_buffer_rsrc_t rw = rsrc(ws + WS_WT, 257 * 8192);
#pragma unroll
                for (int e = 0; e < 2; ++e) {
                    const int k2l = 16 * u + 8 * e + (lane >> 3);
                    const u32x4 o = lds_rd16(L_WS + k2l * 144 + (lane & 7) * 16);
                    st_sc1(o, rw, (64 * cq + k2l) * 8192 + ((jv * 2 + jt) * 64 + 16 * jg + 8 * ja + (lane & 7)) * 16);
                }
                if (u == 0 && lane < 16) __builtin_amdgcn_raw_buffer_store_b16((unsigned short)wsp, rw, 256 * 8192 + 2 * b * 16 + lane * 2, 0, 16);
            }
            if constexpr (s == 5 || s == 6) {
                stage_b(IC<8 * (s - 5)>{}); stage_b(IC<8 * (s - 5) + 1>{}); stage_b(IC<8 * (s - 5) + 2>{}); stage_b(IC<8 * (s - 5) + 3>{});
                stage_b(IC<8 * (s - 5) + 4>{}); stage_b(IC<8 * (s - 5) + 5>{}); stage_b(IC<8 * (s - 5) + 6>{}); stage_b(IC<8 * (s - 5) + 7>{});
            }
            cs = mfma(c0, ones, cs);
            cs = mfma(c1, ones, cs);
        };
        stage(IC<0>{}); stage(IC<1>{}); stage(IC<2>{}); stage(IC<3>{}); stage(IC<4>{}); stage(IC<5>{}); stage(IC<6>{}); stage(IC<7>{});
        if (n == 0 && (typeA || u < 2)) *(LAS f32x4*)(L_CS + (16 * u + 4 * g) * 4) = cs;
    }
    WAIT_LGKM0();
    barrier();
    STAMP(4);
    {
        const unsigned voffw = r8 * 1024 + ((p16 ^ (2 * ((r8 >> 1) & 3))) * 16);
        const char* wb3 = w2 + d0 * 2 + (size_t)u * 32 * 1024;
        const unsigned sd = u * 4096;
        w2_step<0>(wb3, voffw, sd); w2_step<1>(wb3, voffw, sd); w2_step<2>(wb3, voffw, sd); w2_step<3>(wb3, voffw, sd);
    }
    WAIT_VM(16);
    const u64 epoch = epoch_finish(ereq);
    (void)lds_arrive(SY_BOK, lane);
    if (lds_arrive(SY_AARR, lane) == 7) post_replicas(ws, FL_A, b, lane, epoch);
    lds_wait_ge(SY_BOK, 4);
    STAMP(5);
    {
        f32x4 cs = {0, 0, 0, 0};
        const unsigned swz = (q >> 1) | ((g & 1) << 1);
        const unsigned abase = L_W1B + (((8 * g + q) * 128 + swz * 32 + p * 8) ^ (u << 5));
#pragma unroll
        for (int ks = 0; ks < 16; ++ks) { const unsigned a = abase + ks * 4096; cs = mfma(trread2(a, a + 512), ones, cs); }
        if (n == 0) *(LAS f32x4*)(L_CS + (64 + 16 * u + 4 * g) * 4) = cs;
    }
    WAIT_LGKM0();
    (void)lds_arrive(SY_CS2, lane);
    u32x4 C64[2], S64[2], N64[2], C32[2], S32[2], N32[2];
    float tc[8], tsn[8];
    {
        const int m = 16 * u + n;
        const float mf = (float)m;
        {
            const float st = mf * (1.0f / 64.0f), a0 = __builtin_amdgcn_fractf(st * (float)(8 * g));
            float c[8], sn[8];
#pragma unroll
            for (int j = 0; j < 8; ++j) { const float a = a0 + (float)j * st; c[j] = cosr(a); sn[j] = sinr(a); }
            const unsigned sg = (m & 1) ? 0x80008000u : 0u;
#pragma unroll
            for (int e = 0; e < 4; ++e) {
                C64[0][e] = pk2(c[2 * e], c[2 * e + 1]); S64[0][e] = pk2(sn[2 * e], sn[2 * e + 1]); N64[0][e] = S64[0][e] ^ 0x80008000u;
                C64[1][e] = C64[0][e] ^ sg; S64[1][e] = S64[0][e] ^ sg; N64[1][e] = N64[0][e] ^ sg;
            }
        }
        {
            const float st = (float)n * (1.0f / 32.0f), a0 = st * (float)(4 * g);
            float c[4], sn[4];
#pragma unroll
            for (int j = 0; j < 4; ++j) { const float a = a0 + (float)j * st; c[j] = cosr(a); sn[j] = sinr(a); }
            const unsigned sg = (n & 1) ? 0x80008000u : 0u;
#pragma unroll
            for (int e = 0; e < 2; ++e) {
                C32[0][e] = pk2(c[2 * e], c[2 * e + 1]); S32[0][e] = pk2(sn[2 * e], sn[2 * e + 1]);
                C32[0][e + 2] = C32[0][e] ^ sg; S32[0][e + 2] = S32[0][e] ^ sg;
            }
#pragma unroll
            for (int e = 0; e < 4; ++e) {
                C32[1][e] = C32[0][e] ^ 0x80000000u; S32[1][e] = S32[0][e] ^ 0x80000000u;
                N32[0][e] = S32[0][e] ^ 0x80008000u; N32[1][e] = S32[0][e] ^ 0x00008000u;
            }
        }
        {
            const float st = mf * (1.0f / 2048.0f), a0 = st * (float)(4 * g);
#pragma unroll
            for (int j = 0; j < 8; ++j) { const float a = a0 + (float)(16 * (j >> 2) + (j & 3)) * st; tc[j] = cosr(a); tsn[j] = sinr(a); }
        }
#pragma unroll
        for (int e = 0; e < 2; ++e) { PIN4(C64[e]); PIN4(S64[e]); PIN4(N64[e]); PIN4(C32[e]); PIN4(S32[e]); PIN4(N32[e]); }
#pragma unroll
        for (int j = 0; j < 8; ++j) { PIN4(tc[j]); PIN4(tsn[j]); }
    }
    WAIT_VM(0);
    (void)lds_arrive(SY_W2OK, lane);
    STAMP(6);
    __builtin_amdgcn_s_setprio(1);

    const int v = u;
    if (v == 0) {
        poll256(ws, FL_A, b, lane, epoch);
        if (lane == 0) __hip_atomic_store((LAS unsigned*)SY_AOK, 1u, __ATOMIC_RELAXED, __HIP_MEMORY_SCOPE_WORKGROUP);
    } else lds_wait_ge(SY_AOK, 1u);
    STAMP(7);
    u32x4 Ur[2][2], Ui[2][2], Wf[2], Wg[2];
    {
        const __amdgpu_buffer_rsrc_t rx = rsrc(ws + WS_X1T, 512 * 4096), rw = rsrc(ws + WS_WT, 257 * 8192);
#pragma unroll
        for (int mt = 0; mt < 2; ++mt)
#pragma unroll
            for (int ks = 0; ks < 2; ++ks) {
                const unsigned o = b * 8192 + (2 * (16 * mt + n) + ks) * 128 + g * 16;
                Ur[mt][ks] = ld_sc1(rx, o);
                Ui[mt][ks] = ld_sc1(rx, o + 64);
            }
#pragma unroll
        for (int t = 0; t < 2; ++t) {
            Wf[t] = ld_sc1(rw, b * 8192 + ((v * 2 + t) * 64 + lane) * 16);
            if (b == 0) Wg[t] = ld_sc1(rw, 256 * 8192 + ((v * 2 + t) * 64 + lane) * 16);
            else Wg[t] = u32x4{0, 0, 0, 0};
        }
    }
    unsigned sync_n = 0;
    if (b != 0) p2_unit<true>(ws, Ur, Ui, Wf, 2 * b, v, lane, sync_n, C64, S64, N64, C32, S32, N32, tc, tsn);
    else {
        p2_unit<false>(ws, Ur, Ur, Wf, 0, v, lane, sync_n, C64, S64, N64, C32, S32, N32, tc, tsn);
        p2_unit<false>(ws, Ui, Ui, Wg, 1, v, lane, sync_n, C64, S64, N64, C32, S32, N32, tc, tsn);
    }
    STAMP(8);
    WAIT_VM(0);
    if (lds_arrive(SY_ZARR, lane) == 3) post_replicas(ws, FL_Z, b, lane, epoch);
    STAMP(9);
    xv = *(const GAS u32x4*)((const GAS char*)x + ooff);
    u32x4 G[4][4];
#pragma unroll
    for (int db = 0; db < 4; ++db) {
        const int d = d0 + 16 * db + n;
        float c[4], s[4];
#pragma unroll
        for (int jj = 0; jj < 4; ++jj) {
            const float a = (float)(((64 * v + 4 * g + jj) * d) & 511) * (1.0f / 512.0f);
            c[jj] = cosr(a); s[jj] = sinr(a);
        }
        const float ar = (float)((16 * d) & 511) * (1.0f / 512.0f);
        const float cr = cosr(ar), sr = sinr(ar);
#pragma unroll
        for (int ks = 0; ks < 4; ++ks) {
            u32x4 o;
#pragma unroll
            for (int jj = 0; jj < 4; ++jj) o[jj] = pk2(c[jj], -s[jj]);
            if (ks == 0 && v == 0 && g == 0) o[0] = pk2(0.5f, (d & 1) ? -0.5f : 0.5f);
            G[db][ks] = o;
            PIN4(G[db][ks]);
            if (ks < 3) {
#pragma unroll
                for (int jj = 0; jj < 4; ++jj) {
                    const float c2 = c[jj] * cr - s[jj] * sr, s2 = s[jj] * cr + c[jj] * sr;
                    c[jj] = c2; s[jj] = s2;
                }
            }
        }
    }
    STAMP(10);
    if (v == 0) {
        poll256(ws, FL_Z, b, lane, epoch);
        if (lane == 0) {
            __hip_atomic_store((LAS unsigned*)SY_ZOK, 1u, __ATOMIC_RELAXED, __HIP_MEMORY_SCOPE_WORKGROUP);
            if (b == 0) __hip_atomic_store((GAS u64*)(ws + WS_FLAG), epoch, __ATOMIC_RELAXED, __HIP_MEMORY_SCOPE_AGENT);
        }
    } else lds_wait_ge(SY_ZOK, 1u);
    lds_wait_ge(SY_H47, 4);
    STAMP(11);
    const unsigned zreg = L_Z + v * 16384;
    {
        const __amdgpu_buffer_rsrc_t rz = rsrc(ws + WS_ZT, 512 * 4096);
        const int r8 = lane >> 3, ch = lane & 7;
        u32x4 z[16];
#pragma unroll
        for (int i = 0; i < 16; ++i) z[i] = ld_sc1(rz, (128 * v + 8 * i + r8) * 4096 + tile64 * 128 + ch * 16);
#pragma unroll
        for (int i = 0; i < 16; ++i) lds_wr16(zreg + (8 * i + r8) * 128 + ((ch ^ (4 * ((r8 >> 1) & 1))) * 16), z[i]);
    }
    STAMP(12);
    f32x4 acc[4][4];
#pragma unroll
    for (int a = 0; a < 4; ++a)
#pragma unroll
        for (int t = 0; t < 4; ++t) acc[a][t] = f32x4{0, 0, 0, 0};
#pragma unroll
    for (int ks = 0; ks < 4; ++ks)
#pragma unroll
        for (int t = 0; t < 4; ++t) {
            const unsigned a = zreg + (32 * ks + 8 * g + q) * 128 + (((t >> 1) ^ ((q >> 1) & 1)) * 64) + p * 16 + (t & 1) * 8;
            const u32x4 z = trread2(a, a + 512);
#pragma unroll
            for (int db = 0; db < 4; ++db) acc[db][t] = mfma(G[db][ks], z, acc[db][t]);
        }
#pragma unroll
    for (int t = 0; t < 4; ++t)
#pragma unroll
        for (int db = 0; db < 4; ++db) *(LAS f32x4*)(zreg + (t * 4 + db) * 1024 + lane * 16) = acc[db][t];
    STAMP(13);
}

__global__ void __launch_bounds__(512, 2) fused_kernel(const char* x, const unsigned short* wr, const unsigned short* wi,
                                                        const char* __restrict__ w1, const char* __restrict__ w2, char* ws,
                                                        char* __restrict__ out) {
    const int b = blockIdx.x, lane = threadIdx.x & 63, w = __builtin_amdgcn_readfirstlane(threadIdx.x >> 6);
    STAMP(0);
    const int xcd = b & 7, slot = b >> 3;
    const int hf = xcd & 1, P = 4 * (xcd >> 1) + (slot >> 3), i = slot & 7;
    const bool typeA = i < 4;
    const int col0 = 896 * hf + (typeA ? 128 * i : 512 + 96 * (i - 4));
    const int fg0 = 16 * hf + (typeA ? i : 4 + 3 * (i - 4));
    const int rot = (i + 2 * (slot >> 3)) & 7;
    const int tile64 = 4 * xcd + (slot >> 3), d0 = 64 * (slot & 7);
    if (threadIdx.x < 16) *(LAS unsigned*)(unsigned)(L_SY + threadIdx.x * 4) = 0u;
    const int n = lane & 15, g = lane >> 4;
    const int T = w & 3, dp = w >> 2;
    const unsigned tok = 32u * (32 * (T >> 1) + 8 * (n >> 2) + 4 * (T & 1) + (n & 3)) + tile64;
    const unsigned ooff = tok * 1024u + (d0 + 32 * dp + 8 * g) * 2;
    u32x4 xv;
    if (w < 4) m_team(x, w2, ws, b, P, typeA, col0 >> 5, fg0, 8 * hf + i, tile64, d0, rot, w, lane, ooff, xv);
    else s_team(x, w1, w2, wr, wi, ws, b, typeA, col0, fg0, tile64, d0, rot, w - 4, lane, ooff, xv);
    WAIT_LGKM0();
    barrier();
    STAMP(14);
    float y[8], s[8];
#pragma unroll
    for (int j = 0; j < 8; ++j) { y[j] = 0.0f; s[j] = 0.0f; }
#pragma unroll
    for (int e = 0; e < 2; ++e) {
        const unsigned a = (2 * dp + (g >> 1)) * 1024 + (n + 16 * (2 * (g & 1) + e)) * 16;
#pragma unroll
        for (int k = 0; k < 4; ++k) {
            const f32x4 py = *(LAS f32x4*)(a + (T * 4 + k) * 4096), ps = *(LAS f32x4*)(a + L_Z + k * 16384 + T * 4096);
#pragma unroll
            for (int r = 0; r < 4; ++r) { y[4 * e + r] += py[r]; s[4 * e + r] += ps[r]; }
        }
    }
    float o[8];
#pragma unroll
    for (int j = 0; j < 8; ++j) {
        const float yb = bf_round(y[j]);
        const float gg = bf_round(yb / (1.0f + __expf(-yb)));
        const float sb = bf_round(s[j] * (2.0f / 512.0f));
        const float xx = (j & 1) ? bf_hi(xv[j >> 1]) : bf_lo(xv[j >> 1]);
        o[j] = xx + bf_round(sb * gg);
    }
    const u32x4 ov = {pk2(o[0], o[1]), pk2(o[2], o[3]), pk2(o[4], o[5]), pk2(o[6], o[7])};
    *(GAS u32x4*)((GAS char*)out + ooff) = ov;
    STAMP(15);
}

extern "C" void kernel_launch(void* const* d_in, const int* in_sizes, int n_in, void* d_out, int out_size, void* d_ws,
                              size_t ws_size, hipStream_t stream) {
    (void)in_sizes; (void)n_in; (void)out_size; (void)ws_size;
    (void)hipFuncSetAttribute((const void*)fused_kernel, hipFuncAttributeMaxDynamicSharedMemorySize, L_END);
    fused_kernel<<<256, 512, L_END, stream>>>((const char*)d_in[0], (const unsigned short*)d_in[1], (const unsigned short*)d_in[2],
                                              (const char*)d_in[4], (const char*)d_in[5], (char*)d_ws, (char*)d_out);
}
```

```cpp
#include <hip/hip_runtime.h>
#include <stdint.h>
#pragma clang diagnostic ignored "-Wint-to-pointer-cast"

typedef short s16x4 __attribute__((ext_vector_type(4)));
typedef short s16x8 __attribute__((ext_vector_type(8)));
typedef __bf16 bf16x8 __attribute__((ext_vector_type(8)));
typedef __bf16 bf16x2 __attribute__((ext_vector_type(2)));
typedef float f32x4 __attribute__((ext_vector_type(4)));
typedef float f32x2 __attribute__((ext_vector_type(2)));
typedef unsigned u32x4 __attribute__((ext_vector_type(4)));
typedef unsigned u32x2 __attribute__((ext_vector_type(2)));
#define LAS __attribute__((address_space(3)))
#define GAS __attribute__((address_space(1)))
#define DEVI __device__ __forceinline__

constexpr size_t WS_H = 0;
constexpr size_t WS_X1T = 8u << 20;
constexpr size_t WS_WT = 12u << 20;
constexpr size_t WS_ZT = 16u << 20;
constexpr size_t WS_FLAG = 20u << 20;
constexpr size_t WS_TS = 24u << 20;

#define STAMPC(k, id) do { } while (0)
#define STAMP(k, id) do { } while (0)

DEVI unsigned pk2(float a, float b) {
    f32x2 f = {a, b};
    return __builtin_bit_cast(unsigned, __builtin_convertvector(f, bf16x2));
}
DEVI float bf_round(float a) { return __builtin_bit_cast(float, pk2(a, a) << 16); }
DEVI float bf_lo(unsigned u) { return __builtin_bit_cast(float, u << 16); }
DEVI float bf_hi(unsigned u) { return __builtin_bit_cast(float, u & 0xffff0000u); }
DEVI f32x4 mfma(u32x4 a, u32x4 b, f32x4 c) {
    return __builtin_amdgcn_mfma_f32_16x16x32_bf16(__builtin_bit_cast(bf16x8, a), __builtin_bit_cast(bf16x8, b), c, 0, 0, 0);
}
DEVI float dot2(unsigned a, unsigned b, float c) {
    return __builtin_amdgcn_fdot2_f32_bf16(__builtin_bit_cast(bf16x2, a), __builtin_bit_cast(bf16x2, b), c, false);
}
DEVI u32x2 trread(unsigned addr) {
    s16x4 v = __builtin_amdgcn_ds_read_tr16_b64_v4i16((LAS s16x4*)addr);
    return __builtin_bit_cast(u32x2, v);
}
DEVI u32x4 trread2(unsigned a0, unsigned a1) {
    u32x2 lo = trread(a0), hi = trread(a1);
    u32x4 r = {lo[0], lo[1], hi[0], hi[1]};
    return r;
}
DEVI u32x4 lds_rd16(unsigned addr) { return *(LAS u32x4*)addr; }
DEVI void lds_wr16(unsigned addr, u32x4 v) { *(LAS u32x4*)addr = v; }
DEVI void lds_wr8(unsigned addr, u32x2 v) { *(LAS u32x2*)addr = v; }
DEVI float cosr(float rev) { return __builtin_amdgcn_cosf(rev); }
DEVI float sinr(float rev) { return __builtin_amdgcn_sinf(rev); }
DEVI void barrier() { asm volatile("s_barrier" ::: "memory"); }
#define WAIT_VM(n) asm volatile("s_waitcnt vmcnt(%0)" ::"n"(n) : "memory")
#define WAIT_LGKM0() asm volatile("s_waitcnt lgkmcnt(0)" ::: "memory")

DEVI void dma16(unsigned voff, const void* sbase, unsigned dst) {
    unsigned keep;
    asm volatile("s_mov_b32 %0, m0\n\ts_mov_b32 m0, %3\n\ts_nop 0\n\tglobal_load_lds_dwordx4 %1, %2\n\ts_mov_b32 m0, %0"
                 : "=&s"(keep) : "v"(voff), "s"(sbase), "s"(dst) : "memory");
}

constexpr int K1_CS = 147456, K1_ST = K1_CS + 512, K1_LDS = K1_ST + 1024;

template <int NH>
DEVI void k1_consumer(const char* __restrict__ x, char* ws, int P, int w, int lane, int ii0, int fg0) {
    constexpr int NF = 9 - NH;
    constexpr int PITCH = NH * 32;
    constexpr unsigned FOFF = 512 * PITCH;
    const int n = lane & 15, g = lane >> 4, q = n >> 2, p = n & 3;
    const int t0 = 2 * P + (w >> 1);
    const unsigned tl0 = 8 * (n >> 2) + (n & 3);
    const unsigned xoff = (32u * (32 * (w & 1) + tl0) + t0) * 1024u + g * 16;
    const GAS char* xb = (const GAS char*)x;
    u32x4 xr[8][2];
#pragma unroll
    for (int ks = 0; ks < 8; ++ks) {
        xr[ks][0] = *(const GAS u32x4*)(xb + (xoff + ks * 64));
        xr[ks][1] = *(const GAS u32x4*)(xb + (xoff + 131072u + ks * 64));
    }
    const unsigned swz = (NH == 8) ? (q | ((g & 1) << 2)) : ((q >> 1) | ((g & 1) << 1));
    const unsigned abase = (8 * g + q) * PITCH + swz * 32 + p * 8;
    const unsigned fbase = FOFF + lane * 16;
    f32x4 acc[2][9];
#pragma unroll
    for (int i = 0; i < 9; ++i) { acc[0][i] = f32x4{0, 0, 0, 0}; acc[1][i] = f32x4{0, 0, 0, 0}; }
    float sx[2] = {0, 0}, sq[2] = {0, 0};
    u32x4 A[9];
    auto rdA = [&](int ks, int db) -> u32x4 {
        if (db < NH) { const unsigned a = (abase ^ (db << 5)) + ks * 32 * PITCH; return trread2(a, a + 4 * PITCH); }
        else return lds_rd16(fbase + ((db - NH) * 16 + ks) * 1024);
    };
    STAMP(0, 1);
    barrier();
    STAMP(0, 2); STAMPC(0, 14);
#pragma unroll
    for (int db = 0; db < 9; ++db) A[db] = rdA(0, db);
#pragma unroll
    for (int ks = 0; ks < 16; ++ks) {
        if ((ks & 1) && ks < 15) barrier();
        const u32x4 x0 = xr[ks & 7][0], x1 = xr[ks & 7][1];
#pragma unroll
        for (int db = 0; db < 9; ++db) {
            if (db < NH) { acc[0][db] = mfma(A[db], x0, acc[0][db]); acc[1][db] = mfma(A[db], x1, acc[1][db]); }
            else { acc[0][db] = mfma(x0, A[db], acc[0][db]); acc[1][db] = mfma(x1, A[db], acc[1][db]); }
            if (ks < 15) A[db] = rdA(ks + 1, db);
            if (db < 4) {
                sx[0] = dot2(x0[db], 0x3f803f80u, sx[0]); sq[0] = dot2(x0[db], x0[db], sq[0]);
                sx[1] = dot2(x1[db], 0x3f803f80u, sx[1]); sq[1] = dot2(x1[db], x1[db], sq[1]);
            }
            __builtin_amdgcn_sched_barrier(0);
        }
        if (ks < 8) {
            xr[ks][0] = *(const GAS u32x4*)(xb + (xoff + (ks + 8) * 64));
            xr[ks][1] = *(const GAS u32x4*)(xb + (xoff + 131072u + (ks + 8) * 64));
        }
    }
    STAMP(0, 3); STAMPC(0, 15);
    float mb[2], rr[2];
#pragma unroll
    for (int tt = 0; tt < 2; ++tt) {
        float a = sx[tt], b = sq[tt];
        a += __shfl_xor(a, 16); b += __shfl_xor(b, 16);
        a += __shfl_xor(a, 32); b += __shfl_xor(b, 32);
        const float mean = a * (1.0f / 512.0f);
        const float var = fmaxf(b * (1.0f / 512.0f) - mean * mean, 0.0f);
        mb[tt] = bf_round(mean);
        const float den = bf_round(__builtin_sqrtf(bf_round(bf_round(var) + 1e-5f)));
        rr[tt] = 1.0f / den;
        if (g == 0) lds_wr8(K1_ST + (w * 32 + tt * 16 + n) * 8, u32x2{__builtin_bit_cast(unsigned, mb[tt]), __builtin_bit_cast(unsigned, rr[tt])});
    }
    WAIT_LGKM0();
    barrier();
    GAS char* wsb = (GAS char*)ws;
    const int tile64 = 2 * P + (w >> 1);
#pragma unroll
    for (int i2 = 0; i2 < NH / 2; ++i2) {
        const f32x4 c0 = *(LAS f32x4*)(unsigned)(K1_CS + (32 * i2 + 4 * g) * 4), c1 = *(LAS f32x4*)(unsigned)(K1_CS + (32 * i2 + 16 + 4 * g) * 4);
#pragma unroll
        for (int tt = 0; tt < 2; ++tt) {
            float v[8];
#pragma unroll
            for (int r = 0; r < 4; ++r) {
                v[r] = fmaxf(rr[tt] * (acc[tt][2 * i2][r] - mb[tt] * c0[r]), 0.0f);
                v[4 + r] = fmaxf(rr[tt] * (acc[tt][2 * i2 + 1][r] - mb[tt] * c1[r]), 0.0f);
            }
            const u32x4 o = {pk2(v[0], v[1]), pk2(v[2], v[3]), pk2(v[4], v[5]), pk2(v[6], v[7])};
            *(GAS u32x4*)(wsb + WS_H + (size_t)(((tile64 * 56 + ii0 + i2) * 4 + 2 * (w & 1) + tt) * 1024 + lane * 16)) = o;
        }
    }
    f32x4 st[2][2];
#pragma unroll
    for (int tt = 0; tt < 2; ++tt) {
        st[tt][0] = *(LAS f32x4*)(unsigned)(K1_ST + (w * 32 + tt * 16 + 4 * g) * 8);
        st[tt][1] = *(LAS f32x4*)(unsigned)(K1_ST + (w * 32 + tt * 16 + 4 * g) * 8 + 16);
    }
#pragma unroll
    for (int f = 0; f < NF; ++f) {
        const int c = 16 * (fg0 + f) + n;
        const float cs = (c == 0) ? 512.0f : 0.0f;
        float v[8];
#pragma unroll
        for (int tt = 0; tt < 2; ++tt)
#pragma unroll
            for (int r = 0; r < 4; ++r) v[4 * tt + r] = st[tt][r >> 1][2 * (r & 1) + 1] * (acc[tt][NH + f][r] - st[tt][r >> 1][2 * (r & 1)] * cs);
        const u32x4 o = {pk2(v[0], v[1]), pk2(v[2], v[3]), pk2(v[4], v[5]), pk2(v[6], v[7])};
        *(GAS u32x4*)(wsb + WS_X1T + (size_t)(unsigned)(c * 4096 + (128 * P + 32 * w + 8 * g) * 2)) = o;
    }
    STAMP(0, 4);
}

template <int NH>
DEVI void k1_loader(const char* __restrict__ w1, const unsigned short* wr, const unsigned short* wi,
                    char* ws, int b, int u, int lane, int col0, int fg0) {
    constexpr int NF = 9 - NH;
    constexpr int PITCH = NH * 32;
    constexpr unsigned FOFF = 512 * PITCH;
    constexpr int PPS = NH / 2;
    const int n = lane & 15, g = lane >> 4, q = n >> 2, p = n & 3;
    unsigned voff[2];
    if (NH == 8) {
        const int r4 = lane >> 4, p16 = lane & 15;
#pragma unroll
        for (int j = 0; j < 2; ++j) voff[j] = r4 * 3584 + (((p16 >> 1) ^ (r4 | (j << 2))) * 32) + (p16 & 1) * 16;
    } else {
        const int r8 = lane >> 3, p16 = lane & 7;
#pragma unroll
        for (int j = 0; j < 2; ++j) voff[j] = r8 * 3584 + (((p16 >> 1) ^ (((r8 >> 1) & 1) | (j << 1))) * 32) + (p16 & 1) * 16;
    }
    const char* wb = w1 + col0 * 2;
    auto issue = [&](int s) {
#pragma unroll
        for (int j = 0; j < PPS; ++j) {
            const int pi = (NH == 8) ? (16 * s + 4 * u + j) : (8 * s + 2 * u + j);
            const int rows = (NH == 8) ? 4 : 8;
            dma16(voff[(NH == 8) ? ((j >> 1) & 1) : (j & 1)], wb + (size_t)(pi * rows) * 3584, pi * 1024);
        }
    };
    issue(0); issue(1);
    const int rs = b & 63, cq = b >> 6;
    const int jv = rs & 3, ja = (rs >> 2) & 1, jt = (rs >> 3) & 1, jg = rs >> 4;
    unsigned short wv[2][4][2], wsp;
    {
        const GAS unsigned short* wrg = (const GAS unsigned short*)wr;
        const GAS unsigned short* wig = (const GAS unsigned short*)wi;
#pragma unroll
        for (int e = 0; e < 2; ++e)
#pragma unroll
            for (int r = 0; r < 4; ++r) {
                const unsigned k1 = 16 * jv + 8 * ja + 2 * u + e + 64 * (16 * jt + 4 * jg + r);
                const unsigned idx = k1 * 257 + 64 * cq + lane;
                wv[e][r][0] = wrg[idx]; wv[e][r][1] = wig[idx];
            }
        const int unit = 2 * b + ((lane >> 3) & 1), reg = (lane >> 1) & 3;
        const unsigned k1 = 16 * (unit >> 7) + (unit & 15) + 64 * (16 * ((unit >> 6) & 1) + 4 * ((unit >> 4) & 3) + reg);
        wsp = ((lane & 1) ? wig : wrg)[k1 * 257 + 256];
    }
    asm volatile("" ::: "memory");
#pragma unroll
    for (int i = 0; i < NF * 2; ++i) {
        const int bi = u + 4 * i;
        const int ks0 = bi / NF, fgl = bi - ks0 * NF;
        const int c = 16 * (fg0 + fgl) + n;
        const int k2 = (c == 1) ? 256 : (c >> 1), part = (c == 1) ? 0 : (c & 1);
        const int idx0 = (k2 * (32 * ks0 + 8 * g) + 128 * part) & 511;
        const float x0 = (float)idx0 * (1.0f / 512.0f), st = (float)k2 * (1.0f / 512.0f);
        float v[8];
#pragma unroll
        for (int j = 0; j < 8; ++j) v[j] = cosr(x0 + (float)j * st);
        const u32x4 o = {pk2(v[0], v[1]), pk2(v[2], v[3]), pk2(v[4], v[5]), pk2(v[6], v[7])};
        const unsigned sg = (k2 & 1) ? 0x80008000u : 0u;
        const u32x4 o2 = {o[0] ^ sg, o[1] ^ sg, o[2] ^ sg, o[3] ^ sg};
        lds_wr16(FOFF + (fgl * 16 + ks0) * 1024 + lane * 16, o);
        lds_wr16(FOFF + (fgl * 16 + ks0 + 8) * 1024 + lane * 16, o2);
    }
#pragma unroll
    for (int s = 2; s < 8; ++s) issue(s);
    STAMP(0, 1);
    constexpr int NG = NH / 4;
    const unsigned swz = (NH == 8) ? (q | ((g & 1) << 2)) : ((q >> 1) | ((g & 1) << 1));
    const unsigned abase = (8 * g + q) * PITCH + swz * 32 + p * 8;
    float cs[NG];
#pragma unroll
    for (int i = 0; i < NG; ++i) cs[i] = 0.0f;
#pragma unroll
    for (int s = 0; s < 8; ++s) {
        if (s == 0) WAIT_VM(7 * PPS + 17);
        else if (s == 1) WAIT_VM(6 * PPS + 17);
        else if (s == 2) WAIT_VM(5 * PPS);
        else if (s == 3) WAIT_VM(4 * PPS);
        else if (s == 4) WAIT_VM(3 * PPS);
        else if (s == 5) WAIT_VM(2 * PPS);
        else if (s == 6) WAIT_VM(1 * PPS);
        else WAIT_VM(0);
        WAIT_LGKM0();
        barrier();
        if (s == 0) STAMP(0, 2);
#pragma unroll
        for (int kk = 0; kk < 2; ++kk)
#pragma unroll
            for (int i = 0; i < NG; ++i) {
                const unsigned a = (abase ^ ((NG * u + i) << 5)) + (2 * s + kk) * 32 * PITCH;
                const u32x4 f = trread2(a, a + 4 * PITCH);
#pragma unroll
                for (int e = 0; e < 4; ++e) cs[i] = dot2(f[e], 0x3f803f80u, cs[i]);
            }
    }
#pragma unroll
    for (int i = 0; i < NG; ++i) {
        float a = cs[i];
        a += __shfl_xor(a, 16); a += __shfl_xor(a, 32);
        if (g == 0) *(LAS float*)(unsigned)(K1_CS + (16 * (NG * u + i) + n) * 4) = a;
    }
    WAIT_LGKM0();
    barrier();
    GAS char* wsb = (GAS char*)ws;
#pragma unroll
    for (int e = 0; e < 2; ++e) {
        u32x4 o;
#pragma unroll
        for (int r = 0; r < 4; ++r) o[r] = (unsigned)wv[e][r][0] | ((unsigned)wv[e][r][1] << 16);
        const unsigned k2 = 64 * cq + lane;
        *(GAS u32x4*)(wsb + WS_WT + (size_t)(k2 * 8192 + ((jv * 2 + jt) * 64 + 16 * jg + 8 * ja + 2 * u + e) * 16)) = o;
    }
    if (u == 0 && lane < 16) *(GAS unsigned short*)(wsb + WS_WT + (size_t)(256 * 8192 + 2 * b * 16 + lane * 2)) = wsp;
    if (b == 0 && u == 1) *(GAS u32x4*)(wsb + WS_FLAG + lane * 16) = u32x4{0, 0, 0, 0};
    STAMP(0, 4);
}

__global__ void __launch_bounds__(512, 2) k1_kernel(const char* __restrict__ x, const unsigned short* wr,
                                                     const unsigned short* wi, const char* __restrict__ w1,
                                                     char* ws) {
    const int b = blockIdx.x, lane = threadIdx.x & 63, w = __builtin_amdgcn_readfirstlane(threadIdx.x >> 6);
    STAMP(0, 0);
    const int xcd = b & 7, slot = b >> 3;
    const int qd = xcd >> 1, hf = xcd & 1;
    const int P = 4 * qd + (slot >> 3), i = slot & 7;
    if (i < 6) {
        if (w < 4) k1_consumer<8>(x, ws, P, w, lane, 28 * hf + 4 * i, 6 * hf + i);
        else k1_loader<8>(w1, wr, wi, ws, b, w - 4, lane, 896 * hf + 128 * i, 6 * hf + i);
    } else {
        if (w < 4) k1_consumer<4>(x, ws, P, w, lane, 28 * hf + 24 + 2 * (i - 6), 12 + 5 * (2 * hf + i - 6));
        else k1_loader<4>(w1, wr, wi, ws, b, w - 4, lane, 896 * hf + 768 + 64 * (i - 6), 12 + 5 * (2 * hf + i - 6));
    }
}

constexpr int K2_Z = 65536, K2_BP = 131072, K2_ZS = K2_BP + 2 * 64 * 72, K2_SY = K2_ZS + 64 * 144, K2_LDS = K2_SY + 64;
constexpr unsigned SPIN_CAP = 1u << 22;

DEVI unsigned lds_ld_relaxed(unsigned addr) { return __hip_atomic_load((LAS unsigned*)addr, __ATOMIC_RELAXED, __HIP_MEMORY_SCOPE_WORKGROUP); }
DEVI void team_sync(unsigned cnt, unsigned target, int lane) {
    WAIT_LGKM0();
    if (lane == 0) __hip_atomic_fetch_add((LAS unsigned*)cnt, 1u, __ATOMIC_RELAXED, __HIP_MEMORY_SCOPE_WORKGROUP);
#pragma unroll 1
    for (unsigned s = 0; s < SPIN_CAP; ++s) {
        asm volatile("" ::: "memory");
        if ((unsigned)__builtin_amdgcn_readfirstlane(lds_ld_relaxed(cnt)) >= target) break;
        __builtin_amdgcn_s_sleep(1);
    }
    asm volatile("" ::: "memory");
}

#define GLOAD16(dst, voff, sbase, imm) asm volatile("global_load_dwordx4 %0, %1, %2 offset:%3" : "=v"(dst) : "v"(voff), "s"(sbase), "n"(imm) : "memory")

DEVI void k2_team1(const char* __restrict__ w2, char* ws, int tile64, int d0, int w, int lane) {
    const int n = lane & 15, g = lane >> 4, q = n >> 2, p = n & 3;
    const unsigned ring = w * 16384;
    const int r8 = lane >> 3, p16 = lane & 7;
    const unsigned voffw = r8 * 1024 + ((p16 ^ (2 * ((r8 >> 1) & 3))) * 16);
    const char* wb = w2 + d0 * 2 + (size_t)w * 32 * 1024;
    const char* hb = ws + WS_H + (size_t)(tile64 * 56 + w) * 4096;
    const unsigned voffh = lane * 16;
    u32x4 h[4][4];
    auto issue = [&](int i) {
#pragma unroll
        for (int pc = 0; pc < 4; ++pc) dma16(voffw, wb + (size_t)(i * 128 + pc * 8) * 1024, ring + (i & 3) * 4096 + pc * 1024);
        const char* hp = hb + (size_t)i * 16384;
        GLOAD16(h[i & 3][0], voffh, hp, 0); GLOAD16(h[i & 3][1], voffh, hp, 1024);
        GLOAD16(h[i & 3][2], voffh, hp, 2048); GLOAD16(h[i & 3][3], voffh, hp, 3072);
    };
#pragma unroll
    for (int i = 0; i < 4; ++i) issue(i);
    WAIT_LGKM0();
    barrier();
    f32x4 acc[4][4];
#pragma unroll
    for (int a = 0; a < 4; ++a)
#pragma unroll
        for (int t = 0; t < 4; ++t) acc[a][t] = f32x4{0, 0, 0, 0};
    const unsigned abase = ring + (4 * g + q) * 128 + (((2 * g + (q >> 1)) & 3) * 32) + p * 8;
    u32x4 A[2][4];
    auto rdA = [&](int i, u32x4 (&dst)[4]) {
#pragma unroll
        for (int db = 0; db < 4; ++db) { const unsigned a = (abase ^ (db << 5)) + (i & 3) * 4096; dst[db] = trread2(a, a + 2048); }
    };
#define T1_WAIT(N, slot) asm volatile("s_waitcnt vmcnt(%4)" : "+v"(h[slot][0]), "+v"(h[slot][1]), "+v"(h[slot][2]), "+v"(h[slot][3]) : "n"(N) : "memory")
    T1_WAIT(24, 0);
    STAMP(1, 2);
    rdA(0, A[0]);
#pragma unroll
    for (int i = 0; i < 14; ++i) {
        if (i + 1 < 14) {
            if (i + 3 < 14) T1_WAIT(16, (i + 1) & 3);
            else if (i + 2 < 14) T1_WAIT(8, (i + 1) & 3);
            else T1_WAIT(0, (i + 1) & 3);
            rdA(i + 1, A[(i + 1) & 1]);
        }
#pragma unroll
        for (int db = 0; db < 4; ++db)
#pragma unroll
            for (int t = 0; t < 4; ++t) acc[db][t] = mfma(A[i & 1][db], h[i & 3][t], acc[db][t]);
        __builtin_amdgcn_sched_barrier(0);
        if (i + 4 < 14) issue(i + 4);
    }
    STAMP(1, 3);
#pragma unroll
    for (int t = 0; t < 4; ++t)
#pragma unroll
        for (int db = 0; db < 4; ++db) *(LAS f32x4*)(ring + (t * 4 + db) * 1024 + lane * 16) = acc[db][t];
}

template <bool HAS_I>
DEVI void p2_unit(char* ws, const u32x4 (&Ur)[2][2], const u32x4 (&Ui)[2][2], const u32x4 (&Wf)[2], int zrow, int v, int lane, unsigned& sync_n,
                  const u32x4 (&C64)[2], const u32x4 (&S64)[2], const u32x4 (&N64)[2],
                  const u32x4 (&C32)[2], const u32x4 (&S32)[2], const u32x4 (&N32)[2],
                  const float (&tc)[8], const float (&tsn)[8]) {
    const int n = lane & 15, g = lane >> 4, q = n >> 2, p = n & 3;
    float br[8], bi[8];
#pragma unroll
    for (int mt = 0; mt < 2; ++mt) {
        f32x4 dr = {0, 0, 0, 0}, di = {0, 0, 0, 0};
#pragma unroll
        for (int ks = 0; ks < 2; ++ks) {
            dr = mfma(Ur[mt][ks], C64[ks], dr);
            di = mfma(Ur[mt][ks], N64[ks], di);
            if (HAS_I) { dr = mfma(Ui[mt][ks], S64[ks], dr); di = mfma(Ui[mt][ks], C64[ks], di); }
        }
#pragma unroll
        for (int r = 0; r < 4; ++r) {
            const float c = tc[4 * mt + r], s = tsn[4 * mt + r];
            br[4 * mt + r] = dr[r] * c + di[r] * s;
            bi[4 * mt + r] = di[r] * c - dr[r] * s;
        }
    }
    STAMP(1, 10);
    u32x4 Br = {pk2(br[0], br[1]), pk2(br[2], br[3]), pk2(br[4], br[5]), pk2(br[6], br[7])};
    u32x4 Bi = {pk2(bi[0], bi[1]), pk2(bi[2], bi[3]), pk2(bi[4], bi[5]), pk2(bi[6], bi[7])};
    float yr[8], yi[8];
#pragma unroll
    for (int t = 0; t < 2; ++t) {
        f32x4 dr = {0, 0, 0, 0}, di = {0, 0, 0, 0};
        dr = mfma(C32[t], Br, dr); dr = mfma(S32[t], Bi, dr);
        di = mfma(C32[t], Bi, di); di = mfma(N32[t], Br, di);
#pragma unroll
        for (int r = 0; r < 4; ++r) {
            const float wre = bf_lo(Wf[t][r]), wim = bf_hi(Wf[t][r]);
            yr[4 * t + r] = dr[r] * wre - di[r] * wim;
            yi[4 * t + r] = dr[r] * wim + di[r] * wre;
        }
    }
    u32x4 Yr = {pk2(yr[0], yr[1]), pk2(yr[2], yr[3]), pk2(yr[4], yr[5]), pk2(yr[6], yr[7])};
    u32x4 Yi = {pk2(yi[0], yi[1]), pk2(yi[2], yi[3]), pk2(yi[4], yi[5]), pk2(yi[6], yi[7])};
#pragma unroll
    for (int mt = 0; mt < 2; ++mt) {
        f32x4 dr = {0, 0, 0, 0}, di = {0, 0, 0, 0};
        dr = mfma(C32[mt], Yr, dr); dr = mfma(N32[mt], Yi, dr);
        di = mfma(C32[mt], Yi, di); di = mfma(S32[mt], Yr, di);
        float pr[4], pi[4];
#pragma unroll
        for (int r = 0; r < 4; ++r) {
            const float c = tc[4 * mt + r] * (1.0f / 2048.0f), s = tsn[4 * mt + r] * (1.0f / 2048.0f);
            pr[r] = dr[r] * c - di[r] * s;
            pi[r] = di[r] * c + dr[r] * s;
        }
        const unsigned a = K2_BP + (16 * v + n) * 72 + (16 * mt + 4 * g) * 2;
        lds_wr8(a, u32x2{pk2(pr[0], pr[1]), pk2(pr[2], pr[3])});
        lds_wr8(a + 64 * 72, u32x2{pk2(pi[0], pi[1]), pk2(pi[2], pi[3])});
    }
    STAMP(1, 11);
    sync_n += 4; team_sync(K2_SY, sync_n, lane);
    STAMP(1, 12);
#pragma unroll
    for (int nt = 0; nt < 2; ++nt) {
        f32x4 dr = {0, 0, 0, 0}, di = {0, 0, 0, 0};
#pragma unroll
        for (int ks = 0; ks < 2; ++ks) {
            const unsigned a = K2_BP + (32 * ks + 8 * g + q) * 72 + nt * 32 + p * 8;
            const u32x4 pr = trread2(a, a + 4 * 72), pi = trread2(a + 64 * 72, a + 64 * 72 + 4 * 72);
            dr = mfma(C64[ks], pr, dr); dr = mfma(N64[ks], pi, dr);
            if (HAS_I) { di = mfma(C64[ks], pi, di); di = mfma(S64[ks], pr, di); }
        }
        const unsigned a = K2_ZS + (16 * nt + n) * 144 + (16 * v + 4 * g) * 2;
        lds_wr8(a, u32x2{pk2(dr[0], dr[1]), pk2(dr[2], dr[3])});
        if (HAS_I) lds_wr8(a + 32 * 144, u32x2{pk2(di[0], di[1]), pk2(di[2], di[3])});
    }
    sync_n += 4; team_sync(K2_SY, sync_n, lane);
    {
        const __amdgpu_buffer_rsrc_t rs = __builtin_amdgcn_make_buffer_rsrc(ws + WS_ZT, 0, 512 * 4096, 0x00020000);
#pragma unroll
        for (int it = 0; it < (HAS_I ? 2 : 1); ++it) {
            const int row = 8 * v + (lane >> 3), ch = lane & 7;
            const u32x4 o = lds_rd16(K2_ZS + (it * 32 + row) * 144 + ch * 16);
            __builtin_amdgcn_raw_buffer_store_b128(o, rs, (zrow + it) * 4096 + row * 128 + ch * 16, 0, 16);
        }
    }
}

DEVI void k2_team2(char* ws, int b, int tile64, int d0, int v, int lane) {
    const int n = lane & 15, g = lane >> 4, q = n >> 2, p = n & 3;
    u32x4 Ur[2][2], Ui[2][2], Wf[2], Wg[2];
    {
        const GAS char* wsb = (const GAS char*)ws;
#pragma unroll
        for (int mt = 0; mt < 2; ++mt)
#pragma unroll
            for (int ks = 0; ks < 2; ++ks) {
                const unsigned o = 2 * b * 4096 + ((16 * mt + n) * 64 + 32 * ks + 8 * g) * 2;
                Ur[mt][ks] = *(const GAS u32x4*)(wsb + WS_X1T + (size_t)o);
                Ui[mt][ks] = *(const GAS u32x4*)(wsb + WS_X1T + (size_t)(o + 4096));
            }
#pragma unroll
        for (int t = 0; t < 2; ++t) {
            Wf[t] = *(const GAS u32x4*)(wsb + WS_WT + (size_t)(unsigned)(b * 8192 + ((v * 2 + t) * 64 + lane) * 16));
            if (b == 0) Wg[t] = *(const GAS u32x4*)(wsb + WS_WT + (size_t)(unsigned)(256 * 8192 + ((v * 2 + t) * 64 + lane) * 16));
            else Wg[t] = u32x4{0, 0, 0, 0};
        }
    }
    asm volatile("" ::: "memory");
    u32x4 C64[2], S64[2], N64[2], C32[2], S32[2], N32[2];
    float tc[8], tsn[8];
#pragma unroll
    for (int ks = 0; ks < 2; ++ks) {
        float c[8], s[8];
#pragma unroll
        for (int j = 0; j < 8; ++j) {
            const float a = (float)(((16 * v + n) * (32 * ks + 8 * g + j)) & 63) * (1.0f / 64.0f);
            c[j] = cosr(a); s[j] = sinr(a);
        }
#pragma unroll
        for (int e = 0; e < 4; ++e) { C64[ks][e] = pk2(c[2 * e], c[2 * e + 1]); S64[ks][e] = pk2(s[2 * e], s[2 * e + 1]); N64[ks][e] = S64[ks][e] ^ 0x80008000u; }
    }
#pragma unroll
    for (int t = 0; t < 2; ++t) {
        float c[8], s[8];
#pragma unroll
        for (int j = 0; j < 8; ++j) {
            const int kk = (j < 4) ? (4 * g + j) : (16 + 4 * g + j - 4);
            const float a = (float)(((16 * t + n) * kk) & 31) * (1.0f / 32.0f);
            c[j] = cosr(a); s[j] = sinr(a);
        }
#pragma unroll
        for (int e = 0; e < 4; ++e) { C32[t][e] = pk2(c[2 * e], c[2 * e + 1]); S32[t][e] = pk2(s[2 * e], s[2 * e + 1]); N32[t][e] = S32[t][e] ^ 0x80008000u; }
    }
#pragma unroll
    for (int j = 0; j < 8; ++j) {
        const float a = (float)((16 * v + n) * (16 * (j >> 2) + 4 * g + (j & 3))) * (1.0f / 2048.0f);
        tc[j] = cosr(a); tsn[j] = sinr(a);
    }
    WAIT_LGKM0();
    barrier();
    STAMP(1, 2);
    unsigned sync_n = 0;
    if (b != 0) p2_unit<true>(ws, Ur, Ui, Wf, 2 * b, v, lane, sync_n, C64, S64, N64, C32, S32, N32, tc, tsn);
    else {
        p2_unit<false>(ws, Ur, Ur, Wf, 0, v, lane, sync_n, C64, S64, N64, C32, S32, N32, tc, tsn);
        p2_unit<false>(ws, Ui, Ui, Wg, 1, v, lane, sync_n, C64, S64, N64, C32, S32, N32, tc, tsn);
    }
    STAMP(1, 13);
    WAIT_VM(0);
    STAMP(1, 3);
    GAS unsigned* flags = (GAS unsigned*)(ws + WS_FLAG);
    if (lane == 0) {
        const unsigned old = __hip_atomic_fetch_add((LAS unsigned*)(K2_SY + 4), 1u, __ATOMIC_RELAXED, __HIP_MEMORY_SCOPE_WORKGROUP);
        if (old == 3) __hip_atomic_store(flags + b, 1u, __ATOMIC_RELAXED, __HIP_MEMORY_SCOPE_AGENT);
    }
    u32x4 G[4][4];
#pragma unroll
    for (int db = 0; db < 4; ++db)
#pragma unroll
        for (int ks = 0; ks < 4; ++ks) {
            const int d = d0 + 16 * db + n;
            float vv[8];
#pragma unroll
            for (int jj = 0; jj < 4; ++jj) {
                const int k2 = 64 * v + 16 * ks + 4 * g + jj;
                const float a = (float)((k2 * d) & 511) * (1.0f / 512.0f);
                vv[2 * jj] = cosr(a); vv[2 * jj + 1] = -sinr(a);
                if (ks == 0 && jj == 0 && k2 == 0) { vv[0] = 0.5f; vv[1] = (d & 1) ? -0.5f : 0.5f; }
            }
            G[db][ks] = u32x4{pk2(vv[0], vv[1]), pk2(vv[2], vv[3]), pk2(vv[4], vv[5]), pk2(vv[6], vv[7])};
        }
    STAMP(1, 4);
    const __amdgpu_buffer_rsrc_t rz = __builtin_amdgcn_make_buffer_rsrc(ws + WS_ZT, 0, 512 * 4096, 0x00020000);
    if (v == 0) {
        const unsigned foff = lane * 16;
        const char* fb = ws + WS_FLAG;
#pragma unroll 1
        for (unsigned s = 0; s < SPIN_CAP; ++s) {
            u32x4 f;
            asm volatile("global_load_dwordx4 %0, %1, %2 sc1\n\ts_waitcnt vmcnt(0)" : "=v"(f) : "v"(foff), "s"(fb) : "memory");
            if (__all((f[0] & f[1] & f[2] & f[3]) == 1u)) break;
            __builtin_amdgcn_s_sleep(2);
        }
        asm volatile("" ::: "memory");
        if (lane == 0) __hip_atomic_store((LAS unsigned*)(K2_SY + 8), 1u, __ATOMIC_RELAXED, __HIP_MEMORY_SCOPE_WORKGROUP);
    } else {
    #pragma unroll 1
    for (unsigned s = 0; s < SPIN_CAP; ++s) {
            asm volatile("" ::: "memory");
            if ((unsigned)__builtin_amdgcn_readfirstlane(lds_ld_relaxed(K2_SY + 8)) != 0u) break;
            __builtin_amdgcn_s_sleep(2);
        }
    }
    asm volatile("" ::: "memory");
    STAMP(1, 5);
    const unsigned zreg = K2_Z + v * 16384;
    {
        const int r8 = lane >> 3, ch = lane & 7;
        u32x4 z[16];
#pragma unroll
        for (int i = 0; i < 16; ++i) z[i] = __builtin_amdgcn_raw_buffer_load_b128(rz, (128 * v + 8 * i + r8) * 4096 + tile64 * 128 + ch * 16, 0, 16);
#pragma unroll
        for (int i = 0; i < 16; ++i) lds_wr16(zreg + (8 * i + r8) * 128 + ((ch ^ (4 * ((r8 >> 1) & 1))) * 16), z[i]);
    }
    STAMP(1, 6);
    f32x4 acc[4][4];
#pragma unroll
    for (int a = 0; a < 4; ++a)
#pragma unroll
        for (int t = 0; t < 4; ++t) acc[a][t] = f32x4{0, 0, 0, 0};
#pragma unroll
    for (int ks = 0; ks < 4; ++ks)
#pragma unroll
        for (int t = 0; t < 4; ++t) {
            const unsigned a = zreg + (32 * ks + 8 * g + q) * 128 + (((t >> 1) ^ ((q >> 1) & 1)) * 64) + p * 16 + (t & 1) * 8;
            const u32x4 z = trread2(a, a + 512);
#pragma unroll
            for (int db = 0; db < 4; ++db) acc[db][t] = mfma(G[db][ks], z, acc[db][t]);
        }
#pragma unroll
    for (int t = 0; t < 4; ++t)
#pragma unroll
        for (int db = 0; db < 4; ++db) *(LAS f32x4*)(zreg + (t * 4 + db) * 1024 + lane * 16) = acc[db][t];
    STAMP(1, 7);
}

__global__ void __launch_bounds__(512, 2) k2_kernel(const char* __restrict__ x, const char* __restrict__ w2,
                                                     char* ws, char* __restrict__ out) {
    const int b = blockIdx.x, lane = threadIdx.x & 63, w = __builtin_amdgcn_readfirstlane(threadIdx.x >> 6);
    STAMP(1, 0);
    const int xcd = b & 7, slot = b >> 3;
    const int tile64 = 8 * (xcd >> 1) + (slot >> 2), d0 = 64 * (4 * (xcd & 1) + (slot & 3));
    if (threadIdx.x < 4) *(LAS unsigned*)(unsigned)(K2_SY + threadIdx.x * 4) = 0u;
    const int n = lane & 15, g = lane >> 4;
    const int T = w & 3, dp = w >> 2;
    const unsigned tok = 32u * (32 * (T >> 1) + 8 * (n >> 2) + 4 * (T & 1) + (n & 3)) + tile64;
    const unsigned ooff = tok * 1024u + (d0 + 32 * dp + 8 * g) * 2;
    const u32x4 xv = *(const GAS u32x4*)((const GAS char*)x + ooff);
    if (w < 4) k2_team1(w2, ws, tile64, d0, w, lane);
    else k2_team2(ws, b, tile64, d0, w - 4, lane);
    WAIT_LGKM0();
    barrier();
    STAMP(1, 8);
    float y[8], s[8];
#pragma unroll
    for (int j = 0; j < 8; ++j) { y[j] = 0.0f; s[j] = 0.0f; }
#pragma unroll
    for (int e = 0; e < 2; ++e) {
        const unsigned a = (T * 4 + 2 * dp + (g >> 1)) * 1024 + (n + 16 * (2 * (g & 1) + e)) * 16;
#pragma unroll
        for (int k = 0; k < 4; ++k) {
            const f32x4 py = *(LAS f32x4*)(a + k * 16384), ps = *(LAS f32x4*)(a + K2_Z + k * 16384);
#pragma unroll
            for (int r = 0; r < 4; ++r) { y[4 * e + r] += py[r]; s[4 * e + r] += ps[r]; }
        }
    }
    float o[8];
#pragma unroll
    for (int j = 0; j < 8; ++j) {
        const float yb = bf_round(y[j]);
        const float gg = bf_round(yb / (1.0f + __expf(-yb)));
        const float sb = bf_round(s[j] * (2.0f / 512.0f));
        const float xx = (j & 1) ? bf_hi(xv[j >> 1]) : bf_lo(xv[j >> 1]);
        o[j] = xx + bf_round(sb * gg);
    }
    const u32x4 ov = {pk2(o[0], o[1]), pk2(o[2], o[3]), pk2(o[4], o[5]), pk2(o[6], o[7])};
    *(GAS u32x4*)((GAS char*)out + ooff) = ov;
    STAMP(1, 9);
}

extern "C" void kernel_launch(void* const* d_in, const int* in_sizes, int n_in, void* d_out, int out_size, void* d_ws,
                              size_t ws_size, hipStream_t stream) {
    (void)in_sizes; (void)n_in; (void)out_size; (void)ws_size;
    (void)hipFuncSetAttribute((const void*)k1_kernel, hipFuncAttributeMaxDynamicSharedMemorySize, K1_LDS);
    (void)hipFuncSetAttribute((const void*)k2_kernel, hipFuncAttributeMaxDynamicSharedMemorySize, K2_LDS);
    k1_kernel<<<256, 512, K1_LDS, stream>>>((const char*)d_in[0], (const unsigned short*)d_in[1], (const unsigned short*)d_in[2],
                                            (const char*)d_in[4], (char*)d_ws);
    k2_kernel<<<256, 512, K2_LDS, stream>>>((const char*)d_in[0], (const char*)d_in[5], (char*)d_ws, (char*)d_out);
}
```

```cpp
#include <hip/hip_runtime.h>
#include <stdint.h>
#pragma clang diagnostic ignored "-Wint-to-pointer-cast"

typedef short s16x4 __attribute__((ext_vector_type(4)));
typedef __bf16 bf16x8 __attribute__((ext_vector_type(8)));
typedef __bf16 bf16x2 __attribute__((ext_vector_type(2)));
typedef float f32x4 __attribute__((ext_vector_type(4)));
typedef float f32x2 __attribute__((ext_vector_type(2)));
typedef unsigned u32x4 __attribute__((ext_vector_type(4)));
typedef unsigned u32x2 __attribute__((ext_vector_type(2)));
typedef unsigned long long u64;
#define LAS __attribute__((address_space(3)))
#define GAS __attribute__((address_space(1)))
#define DEVI __device__ __forceinline__

constexpr size_t WS_H = 0;
constexpr size_t WS_X1T = 8u << 20;
constexpr size_t WS_WT = 12u << 20;
constexpr size_t WS_ZT = 16u << 20;
constexpr size_t WS_FLAG = 20u << 20;
constexpr size_t WS_TS = 24u << 20;
constexpr unsigned FL_H = 4096, FL_A = 8192, FL_Z = 8192 + 16384;

constexpr unsigned L_W1B = 65536, L_F0 = 131072, L_F1 = 32768;
constexpr unsigned L_CS = 161792, L_ST = L_CS + 512, L_SY = L_ST + 1024, L_END = L_SY + 64;
constexpr unsigned L_Z = 81920, L_BP = 131072, L_ZS = L_BP + 18432, L_WS = 147456;
constexpr unsigned SY_TEAM = L_SY, SY_ZARR = L_SY + 4, SY_ZOK = L_SY + 8, SY_AARR = L_SY + 12, SY_AOK = L_SY + 16, SY_HARR = L_SY + 20,
                   SY_BOK = L_SY + 24, SY_CS2 = L_SY + 28, SY_H47 = L_SY + 32, SY_W2OK = L_SY + 36;
constexpr unsigned SPIN_CAP = 1u << 16;
constexpr unsigned CODE_PIECES = 11;

#define STAMP2(id) do { } while (0)
#define STAMP(id) do { } while (0)

DEVI unsigned pk2(float a, float b) {
    f32x2 f = {a, b};
    return __builtin_bit_cast(unsigned, __builtin_convertvector(f, bf16x2));
}
DEVI float bf_round(float a) { return __builtin_bit_cast(float, pk2(a, a) << 16); }
DEVI float bf_lo(unsigned u) { return __builtin_bit_cast(float, u << 16); }
DEVI float bf_hi(unsigned u) { return __builtin_bit_cast(float, u & 0xffff0000u); }
DEVI f32x4 mfma(u32x4 a, u32x4 b, f32x4 c) {
    return __builtin_amdgcn_mfma_f32_16x16x32_bf16(__builtin_bit_cast(bf16x8, a), __builtin_bit_cast(bf16x8, b), c, 0, 0, 0);
}
DEVI u32x2 trread(unsigned addr) {
    s16x4 v = __builtin_amdgcn_ds_read_tr16_b64_v4i16((LAS s16x4*)addr);
    return __builtin_bit_cast(u32x2, v);
}
DEVI u32x4 trread2(unsigned a0, unsigned a1) {
    u32x2 lo = trread(a0), hi = trread(a1);
    u32x4 r = {lo[0], lo[1], hi[0], hi[1]};
    return r;
}
DEVI u32x4 lds_rd16(unsigned addr) { return *(LAS u32x4*)addr; }
DEVI void lds_wr16(unsigned addr, u32x4 v) { *(LAS u32x4*)addr = v; }
DEVI void lds_wr8(unsigned addr, u32x2 v) { *(LAS u32x2*)addr = v; }
DEVI float cosr(float rev) { return __builtin_amdgcn_cosf(rev); }
DEVI float sinr(float rev) { return __builtin_amdgcn_sinf(rev); }
DEVI void barrier() { asm volatile("s_barrier" ::: "memory"); }
#define WAIT_VM(n) asm volatile("s_waitcnt vmcnt(%0)" ::"n"(n) : "memory")
#define WAIT_LGKM0() asm volatile("s_waitcnt lgkmcnt(0)" ::: "memory")
#define PIN4(a) asm volatile("" : "+v"(a))
DEVI __amdgpu_buffer_rsrc_t rsrc(char* base, unsigned bytes) { return __builtin_amdgcn_make_buffer_rsrc(base, 0, bytes, 0x00020000); }
DEVI void st_sc1(u32x4 v, __amdgpu_buffer_rsrc_t r, unsigned off) { __builtin_amdgcn_raw_buffer_store_b128(v, r, off, 0, 16); }
DEVI u32x4 ld_sc1(__amdgpu_buffer_rsrc_t r, unsigned off) { return __builtin_amdgcn_raw_buffer_load_b128(r, off, 0, 16); }

DEVI void dma16(unsigned voff, const void* sbase, unsigned dst) {
    unsigned keep;
    asm volatile("s_mov_b32 %0, m0\n\ts_mov_b32 m0, %3\n\ts_nop 0\n\tglobal_load_lds_dwordx4 %1, %2\n\ts_mov_b32 m0, %0"
                 : "=&s"(keep) : "v"(voff), "s"(sbase), "s"(dst) : "memory");
}
template <unsigned IMM>
DEVI void dmaq(unsigned voff, const void* sbase, unsigned sdst) {
    asm volatile("s_add_u32 m0, %2, %3\n\ts_nop 0\n\tglobal_load_lds_dwordx4 %0, %1" :: "v"(voff), "s"(sbase), "s"(sdst), "n"(IMM) : "memory", "scc");
}
DEVI unsigned lds_ld_relaxed(unsigned addr) { return __hip_atomic_load((LAS unsigned*)addr, __ATOMIC_RELAXED, __HIP_MEMORY_SCOPE_WORKGROUP); }
DEVI unsigned lds_arrive(unsigned cnt, int lane) {
    unsigned old = 0;
    if (lane == 0) old = __hip_atomic_fetch_add((LAS unsigned*)cnt, 1u, __ATOMIC_RELAXED, __HIP_MEMORY_SCOPE_WORKGROUP);
    return __builtin_amdgcn_readfirstlane(old);
}
DEVI void lds_wait_ge(unsigned cnt, unsigned target) {
#pragma unroll 1
    for (unsigned s = 0; s < SPIN_CAP * 16; ++s) {
        asm volatile("" ::: "memory");
        if ((unsigned)__builtin_amdgcn_readfirstlane(lds_ld_relaxed(cnt)) >= target) break;
        __builtin_amdgcn_s_sleep(1);
    }
    asm volatile("" ::: "memory");
}
DEVI void team_sync(unsigned cnt, unsigned target, int lane) {
    WAIT_LGKM0();
    if (lane == 0) __hip_atomic_fetch_add((LAS unsigned*)cnt, 1u, __ATOMIC_RELAXED, __HIP_MEMORY_SCOPE_WORKGROUP);
    lds_wait_ge(cnt, target);
}
DEVI u64 epoch_request(const char* ws) { return __hip_atomic_load((const GAS u64*)(ws + WS_FLAG), __ATOMIC_RELAXED, __HIP_MEMORY_SCOPE_AGENT); }
DEVI u64 epoch_finish(u64 e) {
    const u64 v = ((u64)(unsigned)__builtin_amdgcn_readfirstlane((unsigned)(e >> 32)) << 32) | (unsigned)__builtin_amdgcn_readfirstlane((unsigned)e);
    return v + 1;
}
DEVI void post_replicas(char* ws, unsigned off, int b, int lane, u64 epoch) {
    if (lane < 8) __hip_atomic_store((GAS u64*)(ws + WS_FLAG + off + lane * 2048 + b * 8), epoch, __ATOMIC_RELAXED, __HIP_MEMORY_SCOPE_AGENT);
}
DEVI void poll256(const char* ws, unsigned off, int b, int lane, u64 epoch) {
    const char* fb = ws + WS_FLAG + off + (b & 7) * 2048;
    const unsigned foff = lane * 16, lo = (unsigned)epoch, hi = (unsigned)(epoch >> 32);
#pragma unroll 1
    for (unsigned s = 0; s < SPIN_CAP; ++s) {
        u32x4 f0, f1;
        asm volatile("global_load_dwordx4 %0, %2, %3 sc1\n\tglobal_load_dwordx4 %1, %2, %3 offset:1024 sc1\n\ts_waitcnt vmcnt(0)"
                     : "=&v"(f0), "=&v"(f1) : "v"(foff), "s"(fb) : "memory");
        const unsigned d = ((f0[0] ^ lo) | (f0[2] ^ lo) | (f1[0] ^ lo) | (f1[2] ^ lo)) | ((f0[1] ^ hi) | (f0[3] ^ hi) | (f1[1] ^ hi) | (f1[3] ^ hi));
        if (__all(d == 0u)) break;
        __builtin_amdgcn_s_sleep(2);
    }
    asm volatile("" ::: "memory");
}

DEVI void h_epilogue(const f32x4& a0, const f32x4& a1, unsigned csa, float mb, float rr, __amdgpu_buffer_rsrc_t rh, unsigned off) {
    const f32x4 c0 = *(LAS f32x4*)(csa), c1 = *(LAS f32x4*)(csa + 64);
    float v[8];
#pragma unroll
    for (int r = 0; r < 4; ++r) {
        v[r] = fmaxf(rr * (a0[r] - mb * c0[r]), 0.0f);
        v[4 + r] = fmaxf(rr * (a1[r] - mb * c1[r]), 0.0f);
    }
    const u32x4 o = {pk2(v[0], v[1]), pk2(v[2], v[3]), pk2(v[4], v[5]), pk2(v[6], v[7])};
    st_sc1(o, rh, off);
}

constexpr int Y_SLOTS = 5;
DEVI void w2_issue(const char* wb, unsigned voffw, int w, int i) {
#pragma unroll
    for (int pc = 0; pc < 4; ++pc) dma16(voffw, wb + (size_t)(i * 128 + pc * 8) * 1024, ((i % Y_SLOTS) * 4 + w) * 4096 + pc * 1024);
}

template <int I>
DEVI void w2_step(const char* wb, unsigned voffw, unsigned sd) {
    const char* sb = wb + (size_t)I * 128 * 1024;
    dmaq<(I % Y_SLOTS) * 16384>(voffw, sb, sd);
    dmaq<(I % Y_SLOTS) * 16384 + 1024>(voffw + 8192, sb, sd);
    dmaq<(I % Y_SLOTS) * 16384 + 2048>(voffw + 16384, sb, sd);
    dmaq<(I % Y_SLOTS) * 16384 + 3072>(voffw + 24576, sb, sd);
}
constexpr int X_AHEAD = 6;
template <int NF, int NG>
DEVI void m_first(const char* x, char* ws, u32x4 (&xr)[16][2], float (&mb)[2], float (&rr)[2], __amdgpu_buffer_rsrc_t rh, unsigned hoff,
                  int P, int w, int lane, int fg0, int rot) {
    const int n = lane & 15, g = lane >> 4, q = n >> 2, p = n & 3;
    const int t0 = 2 * P + (w >> 1);
    const unsigned tl0 = 8 * (n >> 2) + (n & 3);
    const unsigned xoff = (32u * (32 * (w & 1) + tl0) + t0) * 1024u + g * 16;
    const GAS char* xb = (const GAS char*)x;
    auto ldx = [&](int ks) {
        const GAS char* xk = xb + ((((ks >> 1) + rot) & 7) << 7);
        xr[ks][0] = *(const GAS u32x4*)(xk + (xoff + (ks & 1) * 64));
        xr[ks][1] = *(const GAS u32x4*)(xk + (xoff + 131072u + (ks & 1) * 64));
    };
#pragma unroll
    for (int ks = 0; ks < X_AHEAD; ++ks) ldx(ks);
    asm volatile("" ::: "memory");
    f32x4 acc[2][NG], af[2][NF], aS[2], aQ[2];
#pragma unroll
    for (int i = 0; i < NG; ++i) { acc[0][i] = f32x4{0, 0, 0, 0}; acc[1][i] = f32x4{0, 0, 0, 0}; }
#pragma unroll
    for (int f = 0; f < NF; ++f) { af[0][f] = f32x4{0, 0, 0, 0}; af[1][f] = f32x4{0, 0, 0, 0}; }
    aS[0] = aS[1] = aQ[0] = aQ[1] = f32x4{0, 0, 0, 0};
    const u32x4 ones = {0x3f803f80u, 0x3f803f80u, 0x3f803f80u, 0x3f803f80u};
    {
        constexpr unsigned PITCH = (NG == 4) ? 128 : 64;
        const unsigned swz = (NG == 4) ? ((q >> 1) | ((g & 1) << 1)) : (g & 1);
        const unsigned abase = (8 * g + q) * PITCH + swz * 32 + p * 8;
        u32x4 A[NG], F[NF];
        auto rdA = [&](int ks, int db) -> u32x4 { const unsigned a = (abase ^ (db << 5)) + ks * 32 * PITCH; return trread2(a, a + 4 * PITCH); };
        auto rdF = [&](int ks, int f) -> u32x4 { return lds_rd16(((f == 0) ? L_F0 : L_F1 + (f - 1) * 16384) + ks * 1024 + lane * 16); };
        STAMP(1);
        WAIT_LGKM0();
        STAMP2(0);
        barrier();
        STAMP2(8);
        STAMP(2);
#pragma unroll
        for (int db = 0; db < NG; ++db) A[db] = rdA(0, db);
#pragma unroll
        for (int f = 0; f < NF; ++f) F[f] = rdF(0, f);
#pragma unroll
        for (int ks = 0; ks < 16; ++ks) {
            if (ks + X_AHEAD < 16) ldx(ks + X_AHEAD);
            if ((ks & 1) && ks < 15) { STAMP2((ks + 1) / 2); barrier(); STAMP2(8 + (ks + 1) / 2); }
#pragma unroll
            for (int db = 0; db < 4; ++db) {
                if (db < NG) {
                    acc[0][db] = mfma(A[db], xr[ks][0], acc[0][db]);
                    acc[1][db] = mfma(A[db], xr[ks][1], acc[1][db]);
                    if (ks < 15) A[db] = rdA(ks + 1, db);
                }
                if (db < 2) aS[db] = mfma(ones, xr[ks][db], aS[db]);
                else aQ[db - 2] = mfma(xr[ks][db - 2], xr[ks][db - 2], aQ[db - 2]);
                __builtin_amdgcn_sched_barrier(0);
            }
#pragma unroll
            for (int f = 0; f < NF; ++f) {
                af[0][f] = mfma(xr[ks][0], F[f], af[0][f]);
                af[1][f] = mfma(xr[ks][1], F[f], af[1][f]);
                if (ks < 15) F[f] = rdF(ks + 1, f);
                __builtin_amdgcn_sched_barrier(0);
            }
        }
    }
    STAMP(3);
#pragma unroll
    for (int tt = 0; tt < 2; ++tt) {
        const float sq = (p & 2) ? ((p & 1) ? aQ[tt][3] : aQ[tt][2]) : ((p & 1) ? aQ[tt][1] : aQ[tt][0]);
        const float mean = aS[tt][0] * (1.0f / 512.0f);
        const float var = fmaxf(sq * (1.0f / 512.0f) - mean * mean, 0.0f);
        const float m = bf_round(mean);
        const float den = bf_round(__builtin_sqrtf(bf_round(bf_round(var) + 1e-5f)));
        const float r = 1.0f / den;
        if (g == q) lds_wr8(L_ST + (w * 32 + tt * 16 + n) * 8, u32x2{__builtin_bit_cast(unsigned, m), __builtin_bit_cast(unsigned, r)});
    }
    WAIT_LGKM0();
    {
        f32x4 st[2][2];
#pragma unroll
        for (int tt = 0; tt < 2; ++tt) {
            st[tt][0] = *(LAS f32x4*)(L_ST + (w * 32 + tt * 16 + 4 * g) * 8);
            st[tt][1] = *(LAS f32x4*)(L_ST + (w * 32 + tt * 16 + 4 * g) * 8 + 16);
            const f32x2 s = *(LAS f32x2*)(L_ST + (w * 32 + tt * 16 + n) * 8);
            mb[tt] = s[0]; rr[tt] = s[1];
        }
        const __amdgpu_buffer_rsrc_t rx = rsrc(ws + WS_X1T, 512 * 4096);
#pragma unroll
        for (int f = 0; f < NF; ++f) {
            const int c = 16 * (fg0 + f) + n;
            const float cs = (c == 0) ? 512.0f : 0.0f;
            float v[8];
#pragma unroll
            for (int tt = 0; tt < 2; ++tt)
#pragma unroll
                for (int r = 0; r < 4; ++r) v[4 * tt + r] = st[tt][r >> 1][2 * (r & 1) + 1] * (af[tt][f][r] - st[tt][r >> 1][2 * (r & 1)] * cs);
            const u32x4 o = {pk2(v[0], v[1]), pk2(v[2], v[3]), pk2(v[4], v[5]), pk2(v[6], v[7])};
            st_sc1(o, rx, (c >> 1) * 8192 + (4 * P + w) * 128 + (c & 1) * 64 + g * 16);
        }
    }
    STAMP(5);
    barrier();
    STAMP(4);
#pragma unroll
    for (int i2 = 0; i2 < NG / 2; ++i2)
#pragma unroll
        for (int tt = 0; tt < 2; ++tt)
            h_epilogue(acc[tt][2 * i2], acc[tt][2 * i2 + 1], L_CS + (32 * i2 + 4 * g) * 4, mb[tt], rr[tt], rh, hoff + (i2 * 4 + tt) * 1024);
}

DEVI void m_phase_a(const char* x, char* ws, const char* wb, unsigned voffw, int b, int P, bool typeA, int w, int lane, int ii0, int fg0, int hj, int rot, u64& epoch) {
    const int n = lane & 15, g = lane >> 4, q = n >> 2, p = n & 3;
    const u64 ereq = epoch_request(ws);
    u32x4 xr[16][2];
    float mb[2], rr[2];
    const __amdgpu_buffer_rsrc_t rh = rsrc(ws + WS_H, 8u << 20);
    const unsigned hoff = (((2 * P + (w >> 1)) * 56 + ii0) * 4 + 2 * (w & 1)) * 1024 + lane * 16;
    if (typeA) m_first<1, 4>(x, ws, xr, mb, rr, rh, hoff, P, w, lane, fg0, rot);
    else m_first<3, 2>(x, ws, xr, mb, rr, rh, hoff, P, w, lane, fg0, rot);
    if (typeA) WAIT_VM(4); else WAIT_VM(2);
    epoch = epoch_finish(ereq);
    if (lds_arrive(SY_AARR, lane) == 7) post_replicas(ws, FL_A, b, lane, epoch);
    lds_wait_ge(SY_BOK, 4);
    STAMP(6);
    f32x4 a2[2][4];
#pragma unroll
    for (int i = 0; i < 4; ++i) { a2[0][i] = f32x4{0, 0, 0, 0}; a2[1][i] = f32x4{0, 0, 0, 0}; }
    {
        const unsigned swz = (q >> 1) | ((g & 1) << 1);
        const unsigned abase = L_W1B + (8 * g + q) * 128 + swz * 32 + p * 8;
        u32x4 A[4];
        auto rdA = [&](int ks, int db) -> u32x4 { const unsigned a = (abase ^ (db << 5)) + ks * 4096; return trread2(a, a + 512); };
#pragma unroll
        for (int db = 0; db < 4; ++db) A[db] = rdA(0, db);
#pragma unroll
        for (int ks = 0; ks < 16; ++ks) {
#pragma unroll
            for (int db = 0; db < 4; ++db) {
                a2[0][db] = mfma(A[db], xr[ks][0], a2[0][db]);
                a2[1][db] = mfma(A[db], xr[ks][1], a2[1][db]);
                if (ks < 15) A[db] = rdA(ks + 1, db);
                __builtin_amdgcn_sched_barrier(0);
            }
        }
    }
    WAIT_LGKM0();
    (void)lds_arrive(SY_H47, lane);
    STAMP(7);
    lds_wait_ge(SY_CS2, 4);
    const unsigned hoff2 = hoff + (typeA ? 8192 : 4096);
#pragma unroll
    for (int i2 = 0; i2 < 2; ++i2)
#pragma unroll
        for (int tt = 0; tt < 2; ++tt)
            h_epilogue(a2[tt][2 * i2], a2[tt][2 * i2 + 1], L_CS + (64 + 32 * i2 + 4 * g) * 4, mb[tt], rr[tt], rh, hoff2 + (i2 * 4 + tt) * 1024);
    WAIT_VM(0);
    if (lds_arrive(SY_HARR, lane) == 3 && lane == 0)
        __hip_atomic_store((GAS u64*)(ws + WS_FLAG + FL_H + P * 128 + hj * 8), epoch, __ATOMIC_RELAXED, __HIP_MEMORY_SCOPE_AGENT);
    STAMP(8);
}

#define GLOAD16(dst, voff, sbase, imm) asm volatile("global_load_dwordx4 %0, %1, %2 offset:%3 sc1" : "=v"(dst) : "v"(voff), "s"(sbase), "n"(imm) : "memory")
constexpr int y_after(int i) {
    int cnt = 0;
    if (i <= 3) cnt += 4 * (3 - i);
    const int j0 = (i <= 3) ? 0 : (i - 3);
    for (int j = j0; j < i; ++j) { if (j + Y_SLOTS < 14) cnt += 4; if (j + 4 < 14) cnt += 4; }
    return cnt;
}

DEVI void m_team(const char* x, const char* __restrict__ w2, char* ws, int b, int P, bool typeA, int ii0, int fg0, int hj,
                 int tile64, int d0, int rot, int w, int lane, unsigned ooff, u32x4& xv) {
    const int n = lane & 15, g = lane >> 4, q = n >> 2, p = n & 3;
    const int r8 = lane >> 3, p16 = lane & 7;
    const unsigned voffw = r8 * 1024 + ((p16 ^ (2 * ((r8 >> 1) & 3))) * 16);
    const char* wb = w2 + d0 * 2 + (size_t)w * 32 * 1024;
    u64 epoch;
    m_phase_a(x, ws, wb, voffw, b, P, typeA, w, lane, ii0, fg0, hj, rot, epoch);

    lds_wait_ge(SY_H47, 4);
    w2_issue(wb, voffw, w, 4);
    lds_wait_ge(SY_W2OK, 4);
    xv = *(const GAS u32x4*)((const GAS char*)x + ooff);
    {
        const char* fb = ws + WS_FLAG + FL_H + (tile64 >> 1) * 128;
        const unsigned foff = (lane & 15) * 8, lo = (unsigned)epoch, hi = (unsigned)(epoch >> 32);
#pragma unroll 1
        for (unsigned s = 0; s < SPIN_CAP; ++s) {
            u32x2 f;
            asm volatile("global_load_dwordx2 %0, %1, %2 sc1\n\ts_waitcnt vmcnt(0)" : "=v"(f) : "v"(foff), "s"(fb) : "memory");
            if (__all(((f[0] ^ lo) | (f[1] ^ hi)) == 0u)) break;
            __builtin_amdgcn_s_sleep(2);
        }
        asm volatile("" ::: "memory");
    }
    STAMP(9);
    const char* hb = ws + WS_H + (size_t)(tile64 * 56 + w) * 4096;
    const unsigned voffh = lane * 16;
    u32x4 h[4][4];
    auto issue_h = [&](int i) {
        const char* hp = hb + (size_t)i * 16384;
        GLOAD16(h[i & 3][0], voffh, hp, 0); GLOAD16(h[i & 3][1], voffh, hp, 1024);
        GLOAD16(h[i & 3][2], voffh, hp, 2048); GLOAD16(h[i & 3][3], voffh, hp, 3072);
    };
#pragma unroll
    for (int i = 0; i < 4; ++i) issue_h(i);
    f32x4 ay[4][4];
#pragma unroll
    for (int a = 0; a < 4; ++a)
#pragma unroll
        for (int t = 0; t < 4; ++t) ay[a][t] = f32x4{0, 0, 0, 0};
    const unsigned abase = w * 4096 + (4 * g + q) * 128 + (((2 * g + (q >> 1)) & 3) * 32) + p * 8;
    u32x4 A[2][4];
    auto rdA = [&](int i, u32x4 (&dst)[4]) {
#pragma unroll
        for (int db = 0; db < 4; ++db) { const unsigned a = (abase ^ (db << 5)) + (i % Y_SLOTS) * 16384; dst[db] = trread2(a, a + 2048); }
    };
#define T1_WAIT(N, slot) asm volatile("s_waitcnt vmcnt(%4)" : "+v"(h[slot][0]), "+v"(h[slot][1]), "+v"(h[slot][2]), "+v"(h[slot][3]) : "n"(N) : "memory")
    rdA(0, A[0]);
#pragma unroll
    for (int i = 0; i < 14; ++i) {
        T1_WAIT(y_after(i), i & 3);
        if (i == 0) STAMP(10);
        if (i + 1 < 14) rdA(i + 1, A[(i + 1) & 1]);
#pragma unroll
        for (int db = 0; db < 4; ++db)
#pragma unroll
            for (int t = 0; t < 4; ++t) ay[db][t] = mfma(A[i & 1][db], h[i & 3][t], ay[db][t]);
        __builtin_amdgcn_sched_barrier(0);
        if (i + Y_SLOTS < 14) w2_issue(wb, voffw, w, i + Y_SLOTS);
        if (i + 4 < 14) issue_h(i + 4);
    }
    STAMP(11);
#pragma unroll
    for (int t = 0; t < 4; ++t)
#pragma unroll
        for (int db = 0; db < 4; ++db) *(LAS f32x4*)((t * 4 + w) * 4096 + db * 1024 + lane * 16) = ay[db][t];
}

template <bool HAS_I>
DEVI void p2_unit(char* ws, const u32x4 (&Ur)[2][2], const u32x4 (&Ui)[2][2], const u32x4 (&Wf)[2], int zrow, int v, int lane, unsigned& sync_n,
                  const u32x4 (&C64)[2], const u32x4 (&S64)[2], const u32x4 (&N64)[2],
                  const u32x4 (&C32)[2], const u32x4 (&S32)[2], const u32x4 (&N32)[2],
                  const float (&tc)[8], const float (&tsn)[8]) {
    const int n = lane & 15, g = lane >> 4, q = n >> 2, p = n & 3;
    float br[8], bi[8];
#pragma unroll
    for (int mt = 0; mt < 2; ++mt) {
        f32x4 dr = {0, 0, 0, 0}, di = {0, 0, 0, 0};
#pragma unroll
        for (int ks = 0; ks < 2; ++ks) {
            dr = mfma(Ur[mt][ks], C64[ks], dr);
            di = mfma(Ur[mt][ks], N64[ks], di);
            if (HAS_I) { dr = mfma(Ui[mt][ks], S64[ks], dr); di = mfma(Ui[mt][ks], C64[ks], di); }
        }
#pragma unroll
        for (int r = 0; r < 4; ++r) {
            const float c = tc[4 * mt + r], s = tsn[4 * mt + r];
            br[4 * mt + r] = dr[r] * c + di[r] * s;
            bi[4 * mt + r] = di[r] * c - dr[r] * s;
        }
    }
    u32x4 Br = {pk2(br[0], br[1]), pk2(br[2], br[3]), pk2(br[4], br[5]), pk2(br[6], br[7])};
    u32x4 Bi = {pk2(bi[0], bi[1]), pk2(bi[2], bi[3]), pk2(bi[4], bi[5]), pk2(bi[6], bi[7])};
    float yr[8], yi[8];
#pragma unroll
    for (int t = 0; t < 2; ++t) {
        f32x4 dr = {0, 0, 0, 0}, di = {0, 0, 0, 0};
        dr = mfma(C32[t], Br, dr); dr = mfma(S32[t], Bi, dr);
        di = mfma(C32[t], Bi, di); di = mfma(N32[t], Br, di);
#pragma unroll
        for (int r = 0; r < 4; ++r) {
            const float wre = bf_lo(Wf[t][r]), wim = bf_hi(Wf[t][r]);
            yr[4 * t + r] = dr[r] * wre - di[r] * wim;
            yi[4 * t + r] = dr[r] * wim + di[r] * wre;
        }
    }
    u32x4 Yr = {pk2(yr[0], yr[1]), pk2(yr[2], yr[3]), pk2(yr[4], yr[5]), pk2(yr[6], yr[7])};
    u32x4 Yi = {pk2(yi[0], yi[1]), pk2(yi[2], yi[3]), pk2(yi[4], yi[5]), pk2(yi[6], yi[7])};
#pragma unroll
    for (int mt = 0; mt < 2; ++mt) {
        f32x4 dr = {0, 0, 0, 0}, di = {0, 0, 0, 0};
        dr = mfma(C32[mt], Yr, dr); dr = mfma(N32[mt], Yi, dr);
        di = mfma(C32[mt], Yi, di); di = mfma(S32[mt], Yr, di);
        float pr[4], pi[4];
#pragma unroll
        for (int r = 0; r < 4; ++r) {
            const float c = tc[4 * mt + r] * (1.0f / 2048.0f), s = tsn[4 * mt + r] * (1.0f / 2048.0f);
            pr[r] = dr[r] * c - di[r] * s;
            pi[r] = di[r] * c + dr[r] * s;
        }
        const unsigned a = L_BP + (16 * v + n) * 72 + (16 * mt + 4 * g) * 2;
        lds_wr8(a, u32x2{pk2(pr[0], pr[1]), pk2(pr[2], pr[3])});
        lds_wr8(a + 64 * 72, u32x2{pk2(pi[0], pi[1]), pk2(pi[2], pi[3])});
    }
    sync_n += 4; team_sync(SY_TEAM, sync_n, lane);
#pragma unroll
    for (int nt = 0; nt < 2; ++nt) {
        f32x4 dr = {0, 0, 0, 0}, di = {0, 0, 0, 0};
#pragma unroll
        for (int ks = 0; ks < 2; ++ks) {
            const unsigned a = L_BP + (32 * ks + 8 * g + q) * 72 + nt * 32 + p * 8;
            const u32x4 pr = trread2(a, a + 4 * 72), pi = trread2(a + 64 * 72, a + 64 * 72 + 4 * 72);
            dr = mfma(C64[ks], pr, dr); dr = mfma(N64[ks], pi, dr);
            if (HAS_I) { di = mfma(C64[ks], pi, di); di = mfma(S64[ks], pr, di); }
        }
        const unsigned a = L_ZS + (16 * nt + n) * 144 + (16 * v + 4 * g) * 2;
        lds_wr8(a, u32x2{pk2(dr[0], dr[1]), pk2(dr[2], dr[3])});
        if (HAS_I) lds_wr8(a + 32 * 144, u32x2{pk2(di[0], di[1]), pk2(di[2], di[3])});
    }
    sync_n += 4; team_sync(SY_TEAM, sync_n, lane);
    {
        const __amdgpu_buffer_rsrc_t rs = rsrc(ws + WS_ZT, 512 * 4096);
#pragma unroll
        for (int it = 0; it < (HAS_I ? 2 : 1); ++it) {
            const int row = 8 * v + (lane >> 3), ch = lane & 7;
            const u32x4 o = lds_rd16(L_ZS + (it * 32 + row) * 144 + ch * 16);
            st_sc1(o, rs, (zrow + it) * 4096 + row * 128 + ch * 16);
        }
    }
}

#define GLOAD_U16(dst, voff, sbase, imm) asm volatile("global_load_ushort %0, %1, %2 offset:%3" : "=v"(dst) : "v"(voff), "s"(sbase), "n"(imm) : "memory")
template <int S> struct IC { static constexpr int v = S; };
DEVI void f_gen(int fg, unsigned foff, int rot, int u, int lane) {
    const int n = lane & 15, g = lane >> 4;
    const int c = 16 * fg + n;
    const int k2 = (c == 1) ? 256 : (c >> 1), part = (c == 1) ? 0 : (c & 1);
    const float st = (float)k2 * (1.0f / 512.0f), ph = (float)part * 0.25f;
    const unsigned sg = (k2 & 1) ? 0x80008000u : 0u;
    const float d0 = (float)(64 * u + 8 * g + 64 * rot);
#pragma unroll
    for (int e = 0; e < 2; ++e) {
        const float x0 = __builtin_amdgcn_fractf(st * (d0 + 32.0f * e) + ph);
        float v[8];
#pragma unroll
        for (int j = 0; j < 8; ++j) v[j] = cosr(x0 + (float)j * st);
        const u32x4 o = {pk2(v[0], v[1]), pk2(v[2], v[3]), pk2(v[4], v[5]), pk2(v[6], v[7])};
        const u32x4 o2 = {o[0] ^ sg, o[1] ^ sg, o[2] ^ sg, o[3] ^ sg};
        lds_wr16(foff + (2 * u + e) * 1024 + lane * 16, o);
        lds_wr16(foff + (2 * u + e + 8) * 1024 + lane * 16, o2);
    }
}
DEVI void s_team(const char* x, const char* __restrict__ w1, const char* __restrict__ w2, const unsigned short* wr, const unsigned short* wi, char* ws, int b,
                 bool typeA, int col0, int fg0, int tile64, int d0, int rot, int u, int lane, unsigned ooff, u32x4& xv) {
    const int n = lane & 15, g = lane >> 4, q = n >> 2, p = n & 3;
    const u64 ereq = epoch_request(ws);
    const char* wb = w1 + col0 * 2;
    const int r8 = lane >> 3, p16 = lane & 7;
    const unsigned v128 = r8 * 3584 + (p16 & 1) * 16;
    unsigned va[2];
    if (typeA) {
#pragma unroll
        for (int j = 0; j < 2; ++j) va[j] = v128 + (((p16 >> 1) ^ (((r8 >> 1) & 1) | (j << 1))) * 32) + (2 * u + j) * 8 * 3584;
    } else {
        const int r16 = lane >> 2, c4 = lane & 3;
        va[0] = va[1] = r16 * 3584 + (((c4 >> 1) ^ (r16 >> 3)) * 32) + (c4 & 1) * 16 + u * 16 * 3584;
    }
    const unsigned sdA = typeA ? u * 2048 : u * 1024;
    auto stage_a = [&](auto S) {
        constexpr int s = decltype(S)::v;
        const char* sb = wb + (size_t)(((s + rot) & 7) * (64 * 3584));
        if (typeA) { dmaq<s * 8192>(va[0], sb, sdA); dmaq<s * 8192 + 1024>(va[1], sb, sdA); }
        else dmaq<s * 4096>(va[0], sb, sdA);
    };
    stage_a(IC<0>{}); stage_a(IC<1>{});
    const int rs = b & 63, cq = b >> 6;
    const int jv = rs & 3, ja = (rs >> 2) & 1, jt = (rs >> 3) & 1, jg = rs >> 4;
    unsigned wv[2][4][2], wsp;
    {
        const unsigned k0 = 16 * jv + 8 * ja + 2 * u + 64 * (16 * jt + 4 * jg);
        const unsigned off0 = (k0 * 257 + 64 * cq + lane) * 2;
#pragma unroll
        for (int r = 0; r < 4; ++r) {
            const unsigned off = off0 + r * (64 * 514);
            GLOAD_U16(wv[0][r][0], off, wr, 0); GLOAD_U16(wv[0][r][1], off, wi, 0);
            GLOAD_U16(wv[1][r][0], off, wr, 514); GLOAD_U16(wv[1][r][1], off, wi, 514);
        }
        const int unit = 2 * b + ((lane >> 3) & 1), reg = (lane >> 1) & 3;
        const unsigned k1 = 16 * (unit >> 7) + (unit & 15) + 64 * (16 * ((unit >> 6) & 1) + 4 * ((unit >> 4) & 3) + reg);
        const unsigned long long a = (unsigned long long)((lane & 1) ? wi : wr) + (k1 * 257 + 256) * 2;
        asm volatile("global_load_ushort %0, %1, off" : "=v"(wsp) : "v"(a) : "memory");
    }
    stage_a(IC<2>{});
    STAMP(1);
    f_gen(fg0, L_F0, rot, u, lane);
    if (!typeA) { f_gen(fg0 + 1, L_F1, rot, u, lane); f_gen(fg0 + 2, L_F1 + 16384, rot, u, lane); }
    STAMP(2);
    const char* wb2 = wb + (typeA ? 128 : 64);
    const unsigned vbb = v128 + (((p16 >> 1) ^ (((r8 >> 1) & 1) | ((u & 1) << 1))) * 32) + u * 8 * 3584;
    const unsigned sdB = L_W1B + u * 1024;
    auto stage_b = [&](auto K) {
        constexpr int k = decltype(K)::v;
        dmaq<k * 4096>(vbb, wb2 + (size_t)(((k + 2 * rot) & 15) * (32 * 3584)), sdB);
    };
    const u32x4 ones = {0x3f803f80u, 0x3f803f80u, 0x3f803f80u, 0x3f803f80u};
    {
        const unsigned swz = (q >> 1) | ((g & 1) << 1);
        const unsigned abase = typeA ? (((8 * g + q) * 128 + swz * 32 + p * 8) ^ (u << 5)) : ((8 * g + q) * 64 + (((u ^ g) & 1) * 32) + p * 8);
        const unsigned kpitch = typeA ? 4096 : 2048, hpitch = typeA ? 512 : 256;
        f32x4 cs = {0, 0, 0, 0};
        auto stage = [&](auto S) {
            constexpr int s = decltype(S)::v;
            STAMP2(s);
            if (typeA) {
                if (s < 2) WAIT_VM(21); else if (s < 5) WAIT_VM(4); else if (s == 5) WAIT_VM(6); else if (s == 6) WAIT_VM(12); else WAIT_VM(18);
            } else {
                if (s < 2) WAIT_VM(19); else if (s < 5) WAIT_VM(2); else if (s == 5) WAIT_VM(4); else if (s == 6) WAIT_VM(11); else WAIT_VM(18);
            }
            WAIT_LGKM0();
            STAMP2(8 + s);
            barrier();
            if (s == 0) STAMP(3);
            const unsigned a0 = abase + (2 * s) * kpitch, a1 = a0 + kpitch;
            const u32x4 c0 = trread2(a0, a0 + hpitch), c1 = trread2(a1, a1 + hpitch);
            if constexpr (s + 3 < 8) stage_a(IC<s + 3>{});
            if constexpr (s == 2) {
                asm volatile("" : "+v"(wv[0][0][0]), "+v"(wv[0][0][1]), "+v"(wv[0][1][0]), "+v"(wv[0][1][1]), "+v"(wv[0][2][0]), "+v"(wv[0][2][1]), "+v"(wv[0][3][0]), "+v"(wv[0][3][1]),
                                  "+v"(wv[1][0][0]), "+v"(wv[1][0][1]), "+v"(wv[1][1][0]), "+v"(wv[1][1][1]), "+v"(wv[1][2][0]), "+v"(wv[1][2][1]), "+v"(wv[1][3][0]), "+v"(wv[1][3][1]), "+v"(wsp));
#pragma unroll
                for (int e = 0; e < 2; ++e) {
                    u32x4 o;
#pragma unroll
                    for (int r = 0; r < 4; ++r) o[r] = wv[e][r][0] | (wv[e][r][1] << 16);
                    lds_wr16(L_WS + lane * 144 + (2 * u + e) * 16, o);
                }
            }
            if constexpr (s == 4) {
                const __amdgpu_buffer_rsrc_t rw = rsrc(ws + WS_WT, 257 * 8192);
#pragma unroll
                for (int e = 0; e < 2; ++e) {
                    const int k2l = 16 * u + 8 * e + (lane >> 3);
                    const u32x4 o = lds_rd16(L_WS + k2l * 144 + (lane & 7) * 16);
                    st_sc1(o, rw, (64 * cq + k2l) * 8192 + ((jv * 2 + jt) * 64 + 16 * jg + 8 * ja + (lane & 7)) * 16);
                }
                if (u == 0 && lane < 16) __builtin_amdgcn_raw_buffer_store_b16((unsigned short)wsp, rw, 256 * 8192 + 2 * b * 16 + lane * 2, 0, 16);
            }
            if constexpr (s == 5 || s == 6) {
                stage_b(IC<8 * (s - 5)>{}); stage_b(IC<8 * (s - 5) + 1>{}); stage_b(IC<8 * (s - 5) + 2>{}); stage_b(IC<8 * (s - 5) + 3>{});
                stage_b(IC<8 * (s - 5) + 4>{}); stage_b(IC<8 * (s - 5) + 5>{}); stage_b(IC<8 * (s - 5) + 6>{}); stage_b(IC<8 * (s - 5) + 7>{});
            }
            cs = mfma(c0, ones, cs);
            cs = mfma(c1, ones, cs);
        };
        stage(IC<0>{}); stage(IC<1>{}); stage(IC<2>{}); stage(IC<3>{}); stage(IC<4>{}); stage(IC<5>{}); stage(IC<6>{}); stage(IC<7>{});
        if (n == 0 && (typeA || u < 2)) *(LAS f32x4*)(L_CS + (16 * u + 4 * g) * 4) = cs;
    }
    WAIT_LGKM0();
    barrier();
    STAMP(4);
    {
        const unsigned voffw = r8 * 1024 + ((p16 ^ (2 * ((r8 >> 1) & 3))) * 16);
        const char* wb3 = w2 + d0 * 2 + (size_t)u * 32 * 1024;
        const unsigned sd = u * 4096;
        w2_step<0>(wb3, voffw, sd); w2_step<1>(wb3, voffw, sd); w2_step<2>(wb3, voffw, sd); w2_step<3>(wb3, voffw, sd);
    }
    WAIT_VM(16);
    const u64 epoch = epoch_finish(ereq);
    (void)lds_arrive(SY_BOK, lane);
    if (lds_arrive(SY_AARR, lane) == 7) post_replicas(ws, FL_A, b, lane, epoch);
    lds_wait_ge(SY_BOK, 4);
    STAMP(5);
    {
        f32x4 cs = {0, 0, 0, 0};
        const unsigned swz = (q >> 1) | ((g & 1) << 1);
        const unsigned abase = L_W1B + (((8 * g + q) * 128 + swz * 32 + p * 8) ^ (u << 5));
#pragma unroll
        for (int ks = 0; ks < 16; ++ks) { const unsigned a = abase + ks * 4096; cs = mfma(trread2(a, a + 512), ones, cs); }
        if (n == 0) *(LAS f32x4*)(L_CS + (64 + 16 * u + 4 * g) * 4) = cs;
    }
    WAIT_LGKM0();
    (void)lds_arrive(SY_CS2, lane);
    u32x4 C64[2], S64[2], N64[2], C32[2], S32[2], N32[2];
    float tc[8], tsn[8];
    {
        const int m = 16 * u + n;
        const float mf = (float)m;
        {
            const float st = mf * (1.0f / 64.0f), a0 = __builtin_amdgcn_fractf(st * (float)(8 * g));
            float c[8], sn[8];
#pragma unroll
            for (int j = 0; j < 8; ++j) { const float a = a0 + (float)j * st; c[j] = cosr(a); sn[j] = sinr(a); }
            const unsigned sg = (m & 1) ? 0x80008000u : 0u;
#pragma unroll
            for (int e = 0; e < 4; ++e) {
                C64[0][e] = pk2(c[2 * e], c[2 * e + 1]); S64[0][e] = pk2(sn[2 * e], sn[2 * e + 1]); N64[0][e] = S64[0][e] ^ 0x80008000u;
                C64[1][e] = C64[0][e] ^ sg; S64[1][e] = S64[0][e] ^ sg; N64[1][e] = N64[0][e] ^ sg;
            }
        }
        {
            const float st = (float)n * (1.0f / 32.0f), a0 = st * (float)(4 * g);
            float c[4], sn[4];
#pragma unroll
            for (int j = 0; j < 4; ++j) { const float a = a0 + (float)j * st; c[j] = cosr(a); sn[j] = sinr(a); }
            const unsigned sg = (n & 1) ? 0x80008000u : 0u;
#pragma unroll
            for (int e = 0; e < 2; ++e) {
                C32[0][e] = pk2(c[2 * e], c[2 * e + 1]); S32[0][e] = pk2(sn[2 * e], sn[2 * e + 1]);
                C32[0][e + 2] = C32[0][e] ^ sg; S32[0][e + 2] = S32[0][e] ^ sg;
            }
#pragma unroll
            for (int e = 0; e < 4; ++e) {
                C32[1][e] = C32[0][e] ^ 0x80000000u; S32[1][e] = S32[0][e] ^ 0x80000000u;
                N32[0][e] = S32[0][e] ^ 0x80008000u; N32[1][e] = S32[0][e] ^ 0x00008000u;
            }
        }
        {
            const float st = mf * (1.0f / 2048.0f), a0 = st * (float)(4 * g);
#pragma unroll
            for (int j = 0; j < 8; ++j) { const float a = a0 + (float)(16 * (j >> 2) + (j & 3)) * st; tc[j] = cosr(a); tsn[j] = sinr(a); }
        }
#pragma unroll
        for (int e = 0; e < 2; ++e) { PIN4(C64[e]); PIN4(S64[e]); PIN4(N64[e]); PIN4(C32[e]); PIN4(S32[e]); PIN4(N32[e]); }
#pragma unroll
        for (int j = 0; j < 8; ++j) { PIN4(tc[j]); PIN4(tsn[j]); }
    }
    WAIT_VM(0);
    (void)lds_arrive(SY_W2OK, lane);
    STAMP(6);
    __builtin_amdgcn_s_setprio(1);

    const int v = u;
    if (v == 0) {
        poll256(ws, FL_A, b, lane, epoch);
        if (lane == 0) __hip_atomic_store((LAS unsigned*)SY_AOK, 1u, __ATOMIC_RELAXED, __HIP_MEMORY_SCOPE_WORKGROUP);
    } else lds_wait_ge(SY_AOK, 1u);
    STAMP(7);
    u32x4 Ur[2][2], Ui[2][2], Wf[2], Wg[2];
    {
        const __amdgpu_buffer_rsrc_t rx = rsrc(ws + WS_X1T, 512 * 4096), rw = rsrc(ws + WS_WT, 257 * 8192);
#pragma unroll
        for (int mt = 0; mt < 2; ++mt)
#pragma unroll
            for (int ks = 0; ks < 2; ++ks) {
                const unsigned o = b * 8192 + (2 * (16 * mt + n) + ks) * 128 + g * 16;
                Ur[mt][ks] = ld_sc1(rx, o);
                Ui[mt][ks] = ld_sc1(rx, o + 64);
            }
#pragma unroll
        for (int t = 0; t < 2; ++t) {
            Wf[t] = ld_sc1(rw, b * 8192 + ((v * 2 + t) * 64 + lane) * 16);
            if (b == 0) Wg[t] = ld_sc1(rw, 256 * 8192 + ((v * 2 + t) * 64 + lane) * 16);
            else Wg[t] = u32x4{0, 0, 0, 0};
        }
    }
    unsigned sync_n = 0;
    if (b != 0) p2_unit<true>(ws, Ur, Ui, Wf, 2 * b, v, lane, sync_n, C64, S64, N64, C32, S32, N32, tc, tsn);
    else {
        p2_unit<false>(ws, Ur, Ur, Wf, 0, v, lane, sync_n, C64, S64, N64, C32, S32, N32, tc, tsn);
        p2_unit<false>(ws, Ui, Ui, Wg, 1, v, lane, sync_n, C64, S64, N64, C32, S32, N32, tc, tsn);
    }
    STAMP(8);
    WAIT_VM(0);
    if (lds_arrive(SY_ZARR, lane) == 3) post_replicas(ws, FL_Z, b, lane, epoch);
    STAMP(9);
    xv = *(const GAS u32x4*)((const GAS char*)x + ooff);
    u32x4 G[4][4];
#pragma unroll
    for (int db = 0; db < 4; ++db) {
        const int d = d0 + 16 * db + n;
        float c[4], s[4];
#pragma unroll
        for (int jj = 0; jj < 4; ++jj) {
            const float a = (float)(((64 * v + 4 * g + jj) * d) & 511) * (1.0f / 512.0f);
            c[jj] = cosr(a); s[jj] = sinr(a);
        }
        const float ar = (float)((16 * d) & 511) * (1.0f / 512.0f);
        const float cr = cosr(ar), sr = sinr(ar);
#pragma unroll
        for (int ks = 0; ks < 4; ++ks) {
            u32x4 o;
#pragma unroll
            for (int jj = 0; jj < 4; ++jj) o[jj] = pk2(c[jj], -s[jj]);
            if (ks == 0 && v == 0 && g == 0) o[0] = pk2(0.5f, (d & 1) ? -0.5f : 0.5f);
            G[db][ks] = o;
            PIN4(G[db][ks]);
            if (ks < 3) {
#pragma unroll
                for (int jj = 0; jj < 4; ++jj) {
                    const float c2 = c[jj] * cr - s[jj] * sr, s2 = s[jj] * cr + c[jj] * sr;
                    c[jj] = c2; s[jj] = s2;
                }
            }
        }
    }
    STAMP(10);
    if (v == 0) {
        poll256(ws, FL_Z, b, lane, epoch);
        if (lane == 0) {
            __hip_atomic_store((LAS unsigned*)SY_ZOK, 1u, __ATOMIC_RELAXED, __HIP_MEMORY_SCOPE_WORKGROUP);
            if (b == 0) __hip_atomic_store((GAS u64*)(ws + WS_FLAG), epoch, __ATOMIC_RELAXED, __HIP_MEMORY_SCOPE_AGENT);
        }
    } else lds_wait_ge(SY_ZOK, 1u);
    lds_wait_ge(SY_H47, 4);
    STAMP(11);
    const unsigned zreg = L_Z + v * 16384;
    {
        const __amdgpu_buffer_rsrc_t rz = rsrc(ws + WS_ZT, 512 * 4096);
        const int r8 = lane >> 3, ch = lane & 7;
        u32x4 z[16];
#pragma unroll
        for (int i = 0; i < 16; ++i) z[i] = ld_sc1(rz, (128 * v + 8 * i + r8) * 4096 + tile64 * 128 + ch * 16);
#pragma unroll
        for (int i = 0; i < 16; ++i) lds_wr16(zreg + (8 * i + r8) * 128 + ((ch ^ (4 * ((r8 >> 1) & 1))) * 16), z[i]);
    }
    STAMP(12);
    f32x4 acc[4][4];
#pragma unroll
    for (int a = 0; a < 4; ++a)
#pragma unroll
        for (int t = 0; t < 4; ++t) acc[a][t] = f32x4{0, 0, 0, 0};
#pragma unroll
    for (int ks = 0; ks < 4; ++ks)
#pragma unroll
        for (int t = 0; t < 4; ++t) {
            const unsigned a = zreg + (32 * ks + 8 * g + q) * 128 + (((t >> 1) ^ ((q >> 1) & 1)) * 64) + p * 16 + (t & 1) * 8;
            const u32x4 z = trread2(a, a + 512);
#pragma unroll
            for (int db = 0; db < 4; ++db) acc[db][t] = mfma(G[db][ks], z, acc[db][t]);
        }
#pragma unroll
    for (int t = 0; t < 4; ++t)
#pragma unroll
        for (int db = 0; db < 4; ++db) *(LAS f32x4*)(zreg + (t * 4 + db) * 1024 + lane * 16) = acc[db][t];
    STAMP(13);
}

__global__ void __launch_bounds__(512, 2) fused_kernel(const char* x, const unsigned short* wr, const unsigned short* wi,
                                                        const char* __restrict__ w1, const char* __restrict__ w2, char* ws,
                                                        char* __restrict__ out) {
    const int b = blockIdx.x, lane = threadIdx.x & 63, w = __builtin_amdgcn_readfirstlane(threadIdx.x >> 6);
    STAMP(0);
    const int xcd = b & 7, slot = b >> 3;
    if (w == 7 || w == 3) {
        unsigned long long pc;
        asm volatile("s_getpc_b64 %0" : "=s"(pc));
        const unsigned piece = (unsigned)(2 * slot + (w >> 2)) % CODE_PIECES;
        const unsigned voff = piece * 4096 + lane * 64;
        asm volatile("s_mov_b32 m0, %2\n\ts_nop 0\n\tglobal_load_lds_dword %0, %1" :: "v"(voff), "s"(pc), "s"(L_CS) : "memory");
    }
    const int hf = xcd & 1, P = 4 * (xcd >> 1) + (slot >> 3), i = slot & 7;
    const bool typeA = i < 4;
    const int col0 = 896 * hf + (typeA ? 128 * i : 512 + 96 * (i - 4));
    const int fg0 = 16 * hf + (typeA ? i : 4 + 3 * (i - 4));
    const int rot = (i + 2 * (slot >> 3)) & 7;
    const int tile64 = 4 * xcd + (slot >> 3), d0 = 64 * (slot & 7);
    if (threadIdx.x < 16) *(LAS unsigned*)(unsigned)(L_SY + threadIdx.x * 4) = 0u;
    const int n = lane & 15, g = lane >> 4;
    const int T = w & 3, dp = w >> 2;
    const unsigned tok = 32u * (32 * (T >> 1) + 8 * (n >> 2) + 4 * (T & 1) + (n & 3)) + tile64;
    const unsigned ooff = tok * 1024u + (d0 + 32 * dp + 8 * g) * 2;
    u32x4 xv;
    if (w < 4) m_team(x, w2, ws, b, P, typeA, col0 >> 5, fg0, 8 * hf + i, tile64, d0, rot, w, lane, ooff, xv);
    else s_team(x, w1, w2, wr, wi, ws, b, typeA, col0, fg0, tile64, d0, rot, w - 4, lane, ooff, xv);
    WAIT_LGKM0();
    barrier();
    STAMP(14);
    float y[8], s[8];
#pragma unroll
    for (int j = 0; j < 8; ++j) { y[j] = 0.0f; s[j] = 0.0f; }
#pragma unroll
    for (int e = 0; e < 2; ++e) {
        const unsigned a = (2 * dp + (g >> 1)) * 1024 + (n + 16 * (2 * (g & 1) + e)) * 16;
#pragma unroll
        for (int k = 0; k < 4; ++k) {
            const f32x4 py = *(LAS f32x4*)(a + (T * 4 + k) * 4096), ps = *(LAS f32x4*)(a + L_Z + k * 16384 + T * 4096);
#pragma unroll
            for (int r = 0; r < 4; ++r) { y[4 * e + r] += py[r]; s[4 * e + r] += ps[r]; }
        }
    }
    float o[8];
#pragma unroll
    for (int j = 0; j < 8; ++j) {
        const float yb = bf_round(y[j]);
        const float gg = bf_round(yb / (1.0f + __expf(-yb)));
        const float sb = bf_round(s[j] * (2.0f / 512.0f));
        const float xx = (j & 1) ? bf_hi(xv[j >> 1]) : bf_lo(xv[j >> 1]);
        o[j] = xx + bf_round(sb * gg);
    }
    const u32x4 ov = {pk2(o[0], o[1]), pk2(o[2], o[3]), pk2(o[4], o[5]), pk2(o[6], o[7])};
    *(GAS u32x4*)((GAS char*)out + ooff) = ov;
    STAMP(15);
}

extern "C" void kernel_launch(void* const* d_in, const int* in_sizes, int n_in, void* d_out, int out_size, void* d_ws,
                              size_t ws_size, hipStream_t stream) {
    (void)in_sizes; (void)n_in; (void)out_size; (void)ws_size;
    (void)hipFuncSetAttribute((const void*)fused_kernel, hipFuncAttributeMaxDynamicSharedMemorySize, L_END);
    fused_kernel<<<256, 512, L_END, stream>>>((const char*)d_in[0], (const unsigned short*)d_in[1], (const unsigned short*)d_in[2],
                                              (const char*)d_in[4], (const char*)d_in[5], (char*)d_ws, (char*)d_out);
}
```

```cpp
#include <hip/hip_runtime.h>
#include <stdint.h>
#pragma clang diagnostic ignored "-Wint-to-pointer-cast"

typedef short s16x4 __attribute__((ext_vector_type(4)));
typedef __bf16 bf16x8 __attribute__((ext_vector_type(8)));
typedef __bf16 bf16x2 __attribute__((ext_vector_type(2)));
typedef float f32x4 __attribute__((ext_vector_type(4)));
typedef float f32x2 __attribute__((ext_vector_type(2)));
typedef unsigned u32x4 __attribute__((ext_vector_type(4)));
typedef unsigned u32x2 __attribute__((ext_vector_type(2)));
typedef unsigned long long u64;
#define LAS __attribute__((address_space(3)))
#define GAS __attribute__((address_space(1)))
#define DEVI __device__ __forceinline__

constexpr size_t WS_H = 0;
constexpr size_t WS_X1T = 8u << 20;
constexpr size_t WS_WT = 12u << 20;
constexpr size_t WS_ZT = 16u << 20;
constexpr size_t WS_FLAG = 20u << 20;
constexpr size_t WS_TS = 24u << 20;
constexpr unsigned FL_H = 4096, FL_A = 8192, FL_Z = 8192 + 16384;

constexpr unsigned L_W1B = 65536, L_F0 = 131072, L_F1 = 32768;
constexpr unsigned L_CS = 161792, L_ST = L_CS + 512, L_SY = L_ST + 1024, L_END = L_SY + 64;
constexpr unsigned L_Z = 81920, L_BP = 131072, L_ZS = L_BP + 18432, L_WS = 147456;
constexpr unsigned SY_TEAM = L_SY, SY_ZARR = L_SY + 4, SY_ZOK = L_SY + 8, SY_AARR = L_SY + 12, SY_AOK = L_SY + 16, SY_HARR = L_SY + 20,
                   SY_BOK = L_SY + 24, SY_CS2 = L_SY + 28, SY_H47 = L_SY + 32, SY_W2OK = L_SY + 36, SY_Y6 = L_SY + 40;
constexpr unsigned SPIN_CAP = 1u << 16;
constexpr unsigned CODE_PIECES = 11;

#define STAMP2(id) do { } while (0)
#define STAMP3(id) do { } while (0)
#define STAMP(id) do { } while (0)

DEVI unsigned pk2(float a, float b) {
    f32x2 f = {a, b};
    return __builtin_bit_cast(unsigned, __builtin_convertvector(f, bf16x2));
}
DEVI float bf_round(float a) { return __builtin_bit_cast(float, pk2(a, a) << 16); }
DEVI float bf_lo(unsigned u) { return __builtin_bit_cast(float, u << 16); }
DEVI float bf_hi(unsigned u) { return __builtin_bit_cast(float, u & 0xffff0000u); }
DEVI f32x4 mfma(u32x4 a, u32x4 b, f32x4 c) {
    return __builtin_amdgcn_mfma_f32_16x16x32_bf16(__builtin_bit_cast(bf16x8, a), __builtin_bit_cast(bf16x8, b), c, 0, 0, 0);
}
DEVI u32x2 trread(unsigned addr) {
    s16x4 v = __builtin_amdgcn_ds_read_tr16_b64_v4i16((LAS s16x4*)addr);
    return __builtin_bit_cast(u32x2, v);
}
DEVI u32x4 trread2(unsigned a0, unsigned a1) {
    u32x2 lo = trread(a0), hi = trread(a1);
    u32x4 r = {lo[0], lo[1], hi[0], hi[1]};
    return r;
}
DEVI u32x4 lds_rd16(unsigned addr) { return *(LAS u32x4*)addr; }
DEVI void lds_wr16(unsigned addr, u32x4 v) { *(LAS u32x4*)addr = v; }
DEVI void lds_wr8(unsigned addr, u32x2 v) { *(LAS u32x2*)addr = v; }
DEVI float cosr(float rev) { return __builtin_amdgcn_cosf(rev); }
DEVI float sinr(float rev) { return __builtin_amdgcn_sinf(rev); }
DEVI void barrier() { asm volatile("s_barrier" ::: "memory"); }
#define WAIT_VM(n) asm volatile("s_waitcnt vmcnt(%0)" ::"n"(n) : "memory")
#define WAIT_LGKM0() asm volatile("s_waitcnt lgkmcnt(0)" ::: "memory")
#define PIN4(a) asm volatile("" : "+v"(a))
DEVI __amdgpu_buffer_rsrc_t rsrc(char* base, unsigned bytes) { return __builtin_amdgcn_make_buffer_rsrc(base, 0, bytes, 0x00020000); }
DEVI void st_sc1(u32x4 v, __amdgpu_buffer_rsrc_t r, unsigned off) { __builtin_amdgcn_raw_buffer_store_b128(v, r, off, 0, 16); }
DEVI u32x4 ld_sc1(__amdgpu_buffer_rsrc_t r, unsigned off) { return __builtin_amdgcn_raw_buffer_load_b128(r, off, 0, 16); }

DEVI void dma16(unsigned voff, const void* sbase, unsigned dst) {
    unsigned keep;
    asm volatile("s_mov_b32 %0, m0\n\ts_mov_b32 m0, %3\n\ts_nop 0\n\tglobal_load_lds_dwordx4 %1, %2\n\ts_mov_b32 m0, %0"
                 : "=&s"(keep) : "v"(voff), "s"(sbase), "s"(dst) : "memory");
}
template <unsigned IMM>
DEVI void dmaq(unsigned voff, const void* sbase, unsigned sdst) {
    asm volatile("s_add_u32 m0, %2, %3\n\ts_nop 0\n\tglobal_load_lds_dwordx4 %0, %1" :: "v"(voff), "s"(sbase), "s"(sdst), "n"(IMM) : "memory", "scc");
}
DEVI unsigned lds_ld_relaxed(unsigned addr) { return __hip_atomic_load((LAS unsigned*)addr, __ATOMIC_RELAXED, __HIP_MEMORY_SCOPE_WORKGROUP); }
DEVI unsigned lds_arrive(unsigned cnt, int lane) {
    unsigned old = 0;
    if (lane == 0) old = __hip_atomic_fetch_add((LAS unsigned*)cnt, 1u, __ATOMIC_RELAXED, __HIP_MEMORY_SCOPE_WORKGROUP);
    return __builtin_amdgcn_readfirstlane(old);
}
DEVI void lds_wait_ge(unsigned cnt, unsigned target) {
#pragma unroll 1
    for (unsigned s = 0; s < SPIN_CAP * 16; ++s) {
        asm volatile("" ::: "memory");
        if ((unsigned)__builtin_amdgcn_readfirstlane(lds_ld_relaxed(cnt)) >= target) break;
        __builtin_amdgcn_s_sleep(1);
    }
    asm volatile("" ::: "memory");
}
DEVI void team_sync(unsigned cnt, unsigned target, int lane) {
    WAIT_LGKM0();
    if (lane == 0) __hip_atomic_fetch_add((LAS unsigned*)cnt, 1u, __ATOMIC_RELAXED, __HIP_MEMORY_SCOPE_WORKGROUP);
    lds_wait_ge(cnt, target);
}
DEVI u64 epoch_request(const char* ws) { return __hip_atomic_load((const GAS u64*)(ws + WS_FLAG), __ATOMIC_RELAXED, __HIP_MEMORY_SCOPE_AGENT); }
DEVI u64 epoch_finish(u64 e) {
    const u64 v = ((u64)(unsigned)__builtin_amdgcn_readfirstlane((unsigned)(e >> 32)) << 32) | (unsigned)__builtin_amdgcn_readfirstlane((unsigned)e);
    return v + 1;
}
DEVI void post_replicas(char* ws, unsigned off, int b, int lane, u64 epoch) {
    if (lane < 8) __hip_atomic_store((GAS u64*)(ws + WS_FLAG + off + lane * 2048 + b * 8), epoch, __ATOMIC_RELAXED, __HIP_MEMORY_SCOPE_AGENT);
}
DEVI void poll256(const char* ws, unsigned off, int b, int lane, u64 epoch) {
    const char* fb = ws + WS_FLAG + off + (b & 7) * 2048;
    const unsigned foff = lane * 16, lo = (unsigned)epoch, hi = (unsigned)(epoch >> 32);
#pragma unroll 1
    for (unsigned s = 0; s < SPIN_CAP; ++s) {
        u32x4 f0, f1;
        asm volatile("global_load_dwordx4 %0, %2, %3 sc1\n\tglobal_load_dwordx4 %1, %2, %3 offset:1024 sc1\n\ts_waitcnt vmcnt(0)"
                     : "=&v"(f0), "=&v"(f1) : "v"(foff), "s"(fb) : "memory");
        const unsigned d = ((f0[0] ^ lo) | (f0[2] ^ lo) | (f1[0] ^ lo) | (f1[2] ^ lo)) | ((f0[1] ^ hi) | (f0[3] ^ hi) | (f1[1] ^ hi) | (f1[3] ^ hi));
        if (__all(d == 0u)) break;
        __builtin_amdgcn_s_sleep(2);
    }
    asm volatile("" ::: "memory");
}

DEVI void h_epilogue(const f32x4& a0, const f32x4& a1, unsigned csa, float mb, float rr, __amdgpu_buffer_rsrc_t rh, unsigned off) {
    const f32x4 c0 = *(LAS f32x4*)(csa), c1 = *(LAS f32x4*)(csa + 64);
    float v[8];
#pragma unroll
    for (int r = 0; r < 4; ++r) {
        v[r] = fmaxf(rr * (a0[r] - mb * c0[r]), 0.0f);
        v[4 + r] = fmaxf(rr * (a1[r] - mb * c1[r]), 0.0f);
    }
    const u32x4 o = {pk2(v[0], v[1]), pk2(v[2], v[3]), pk2(v[4], v[5]), pk2(v[6], v[7])};
    st_sc1(o, rh, off);
}

template <int S> struct IC { static constexpr int v = S; };
constexpr int H_RING = 6;
DEVI unsigned y_blk(int i, unsigned yrot) { const unsigned t = yrot + i; return (t >= 14) ? t - 14 : t; }
constexpr int y_slot(int i) { return i < 4 ? i : i < 7 ? i + 1 : i < 12 ? i - 7 : i - 12; }
template <int I>
DEVI void w2_step(const char* wb, unsigned voffw, unsigned sd, unsigned yrot) {
    const char* sb = wb + (size_t)(y_blk(I, yrot) * (128u * 1024u));
    dmaq<y_slot(I) * 16384>(voffw, sb, sd);
    dmaq<y_slot(I) * 16384 + 1024>(voffw + 8192, sb, sd);
    dmaq<y_slot(I) * 16384 + 2048>(voffw + 16384, sb, sd);
    dmaq<y_slot(I) * 16384 + 3072>(voffw + 24576, sb, sd);
}
constexpr int X_AHEAD = 6;
template <int NF, int NG>
DEVI void m_first(const char* x, char* ws, u32x4 (&xr)[16][2], float (&mb)[2], float (&rr)[2], __amdgpu_buffer_rsrc_t rh, unsigned hoff,
                  int P, int w, int lane, int fg0, int rot) {
    const int n = lane & 15, g = lane >> 4, q = n >> 2, p = n & 3;
    const int t0 = 2 * P + (w >> 1);
    const unsigned tl0 = 8 * (n >> 2) + (n & 3);
    const unsigned xoff = (32u * (32 * (w & 1) + tl0) + t0) * 1024u + g * 16;
    const GAS char* xb = (const GAS char*)x;
    auto ldx = [&](int ks) {
        const GAS char* xk = xb + ((((ks >> 1) + rot) & 7) << 7);
        xr[ks][0] = *(const GAS u32x4*)(xk + (xoff + (ks & 1) * 64));
        xr[ks][1] = *(const GAS u32x4*)(xk + (xoff + 131072u + (ks & 1) * 64));
    };
#pragma unroll
    for (int ks = 0; ks < X_AHEAD; ++ks) ldx(ks);
    asm volatile("" ::: "memory");
    f32x4 acc[2][NG], af[2][NF], aS[2], aQ[2];
#pragma unroll
    for (int i = 0; i < NG; ++i) { acc[0][i] = f32x4{0, 0, 0, 0}; acc[1][i] = f32x4{0, 0, 0, 0}; }
#pragma unroll
    for (int f = 0; f < NF; ++f) { af[0][f] = f32x4{0, 0, 0, 0}; af[1][f] = f32x4{0, 0, 0, 0}; }
    aS[0] = aS[1] = aQ[0] = aQ[1] = f32x4{0, 0, 0, 0};
    const u32x4 ones = {0x3f803f80u, 0x3f803f80u, 0x3f803f80u, 0x3f803f80u};
    {
        constexpr unsigned PITCH = (NG == 4) ? 128 : 64;
        const unsigned swz = (NG == 4) ? ((q >> 1) | ((g & 1) << 1)) : (g & 1);
        const unsigned abase = (8 * g + q) * PITCH + swz * 32 + p * 8;
        u32x4 A[NG], F[NF];
        auto rdA = [&](int ks, int db) -> u32x4 { const unsigned a = (abase ^ (db << 5)) + ks * 32 * PITCH; return trread2(a, a + 4 * PITCH); };
        auto rdF = [&](int ks, int f) -> u32x4 { return lds_rd16(((f == 0) ? L_F0 : L_F1 + (f - 1) * 16384) + ks * 1024 + lane * 16); };
        STAMP(1);
        WAIT_LGKM0();
        STAMP2(0);
        barrier();
        STAMP2(8);
        STAMP(2);
#pragma unroll
        for (int db = 0; db < NG; ++db) A[db] = rdA(0, db);
#pragma unroll
        for (int f = 0; f < NF; ++f) F[f] = rdF(0, f);
#pragma unroll
        for (int ks = 0; ks < 16; ++ks) {
            if (ks + X_AHEAD < 16) ldx(ks + X_AHEAD);
            if ((ks & 1) && ks < 15) { STAMP2((ks + 1) / 2); barrier(); STAMP2(8 + (ks + 1) / 2); }
#pragma unroll
            for (int db = 0; db < 4; ++db) {
                if (db < NG) {
                    acc[0][db] = mfma(A[db], xr[ks][0], acc[0][db]);
                    acc[1][db] = mfma(A[db], xr[ks][1], acc[1][db]);
                    if (ks < 15) A[db] = rdA(ks + 1, db);
                }
                if (db < 2) aS[db] = mfma(ones, xr[ks][db], aS[db]);
                else aQ[db - 2] = mfma(xr[ks][db - 2], xr[ks][db - 2], aQ[db - 2]);
                __builtin_amdgcn_sched_barrier(0);
            }
#pragma unroll
            for (int f = 0; f < NF; ++f) {
                af[0][f] = mfma(xr[ks][0], F[f], af[0][f]);
                af[1][f] = mfma(xr[ks][1], F[f], af[1][f]);
                if (ks < 15) F[f] = rdF(ks + 1, f);
                __builtin_amdgcn_sched_barrier(0);
            }
        }
    }
    STAMP(3);
#pragma unroll
    for (int tt = 0; tt < 2; ++tt) {
        const float sq = (p & 2) ? ((p & 1) ? aQ[tt][3] : aQ[tt][2]) : ((p & 1) ? aQ[tt][1] : aQ[tt][0]);
        const float mean = aS[tt][0] * (1.0f / 512.0f);
        const float var = fmaxf(sq * (1.0f / 512.0f) - mean * mean, 0.0f);
        const float m = bf_round(mean);
        const float den = bf_round(__builtin_sqrtf(bf_round(bf_round(var) + 1e-5f)));
        const float r = 1.0f / den;
        if (g == q) lds_wr8(L_ST + (w * 32 + tt * 16 + n) * 8, u32x2{__builtin_bit_cast(unsigned, m), __builtin_bit_cast(unsigned, r)});
    }
    WAIT_LGKM0();
    {
        f32x4 st[2][2];
#pragma unroll
        for (int tt = 0; tt < 2; ++tt) {
            st[tt][0] = *(LAS f32x4*)(L_ST + (w * 32 + tt * 16 + 4 * g) * 8);
            st[tt][1] = *(LAS f32x4*)(L_ST + (w * 32 + tt * 16 + 4 * g) * 8 + 16);
            const f32x2 s = *(LAS f32x2*)(L_ST + (w * 32 + tt * 16 + n) * 8);
            mb[tt] = s[0]; rr[tt] = s[1];
        }
        const __amdgpu_buffer_rsrc_t rx = rsrc(ws + WS_X1T, 512 * 4096);
#pragma unroll
        for (int f = 0; f < NF; ++f) {
            const int c = 16 * (fg0 + f) + n;
            const float cs = (c == 0) ? 512.0f : 0.0f;
            float v[8];
#pragma unroll
            for (int tt = 0; tt < 2; ++tt)
#pragma unroll
                for (int r = 0; r < 4; ++r) v[4 * tt + r] = st[tt][r >> 1][2 * (r & 1) + 1] * (af[tt][f][r] - st[tt][r >> 1][2 * (r & 1)] * cs);
            const u32x4 o = {pk2(v[0], v[1]), pk2(v[2], v[3]), pk2(v[4], v[5]), pk2(v[6], v[7])};
            st_sc1(o, rx, (c >> 1) * 8192 + (4 * P + w) * 128 + (c & 1) * 64 + g * 16);
        }
    }
    STAMP(5);
    barrier();
    STAMP(4);
#pragma unroll
    for (int i2 = 0; i2 < NG / 2; ++i2)
#pragma unroll
        for (int tt = 0; tt < 2; ++tt)
            h_epilogue(acc[tt][2 * i2], acc[tt][2 * i2 + 1], L_CS + (32 * i2 + 4 * g) * 4, mb[tt], rr[tt], rh, hoff + (i2 * 4 + tt) * 1024);
}

DEVI void m_phase_a(const char* x, char* ws, const char* wb, unsigned voffw, int b, int P, bool typeA, int w, int lane, int ii0, int fg0, int hj, int rot, u64& epoch) {
    const int n = lane & 15, g = lane >> 4, q = n >> 2, p = n & 3;
    const u64 ereq = epoch_request(ws);
    u32x4 xr[16][2];
    float mb[2], rr[2];
    const __amdgpu_buffer_rsrc_t rh = rsrc(ws + WS_H, 8u << 20);
    const unsigned hoff = (((2 * P + (w >> 1)) * 56 + ii0) * 4 + 2 * (w & 1)) * 1024 + lane * 16;
    if (typeA) m_first<1, 4>(x, ws, xr, mb, rr, rh, hoff, P, w, lane, fg0, rot);
    else m_first<3, 2>(x, ws, xr, mb, rr, rh, hoff, P, w, lane, fg0, rot);
    if (typeA) WAIT_VM(4); else WAIT_VM(2);
    epoch = epoch_finish(ereq);
    if (lds_arrive(SY_AARR, lane) == 7) post_replicas(ws, FL_A, b, lane, epoch);
    lds_wait_ge(SY_BOK, 4);
    STAMP(6);
    f32x4 a2[2][4];
#pragma unroll
    for (int i = 0; i < 4; ++i) { a2[0][i] = f32x4{0, 0, 0, 0}; a2[1][i] = f32x4{0, 0, 0, 0}; }
    {
        const unsigned swz = (q >> 1) | ((g & 1) << 1);
        const unsigned abase = L_W1B + (8 * g + q) * 128 + swz * 32 + p * 8;
        u32x4 A[4];
        auto rdA = [&](int ks, int db) -> u32x4 { const unsigned a = (abase ^ (db << 5)) + ks * 4096; return trread2(a, a + 512); };
#pragma unroll
        for (int db = 0; db < 4; ++db) A[db] = rdA(0, db);
#pragma unroll
        for (int ks = 0; ks < 16; ++ks) {
#pragma unroll
            for (int db = 0; db < 4; ++db) {
                a2[0][db] = mfma(A[db], xr[ks][0], a2[0][db]);
                a2[1][db] = mfma(A[db], xr[ks][1], a2[1][db]);
                if (ks < 15) A[db] = rdA(ks + 1, db);
                __builtin_amdgcn_sched_barrier(0);
            }
        }
    }
    WAIT_LGKM0();
    (void)lds_arrive(SY_H47, lane);
    STAMP(7);
    lds_wait_ge(SY_CS2, 4);
    const unsigned hoff2 = hoff + (typeA ? 8192 : 4096);
#pragma unroll
    for (int i2 = 0; i2 < 2; ++i2)
#pragma unroll
        for (int tt = 0; tt < 2; ++tt)
            h_epilogue(a2[tt][2 * i2], a2[tt][2 * i2 + 1], L_CS + (64 + 32 * i2 + 4 * g) * 4, mb[tt], rr[tt], rh, hoff2 + (i2 * 4 + tt) * 1024);
    WAIT_VM(0);
    if (lds_arrive(SY_HARR, lane) == 3 && lane == 0)
        __hip_atomic_store((GAS u64*)(ws + WS_FLAG + FL_H + P * 128 + hj * 8), epoch, __ATOMIC_RELAXED, __HIP_MEMORY_SCOPE_AGENT);
    STAMP(8);
}

#define GLOAD16(dst, voff, sbase, imm) asm volatile("global_load_dwordx4 %0, %1, %2 offset:%3 sc1" : "=v"(dst) : "v"(voff), "s"(sbase), "n"(imm) : "memory")
constexpr int y_ops(int j) { return ((j == 4 || j == 5) ? 4 : 0) + ((j + H_RING < 14) ? 4 : 0); }
constexpr int y_after(int i) {
    int cnt = (i < H_RING) ? 4 * (H_RING - 1 - i) : 0;
    for (int j = (i < H_RING) ? 0 : (i - (H_RING - 1)); j < i; ++j) cnt += y_ops(j);
    if (i == 8 || i == 9) {
        int c2 = (i - 4 + H_RING < 14) ? 4 : 0;
        for (int j = i - 3; j < i; ++j) c2 += y_ops(j);
        if (c2 < cnt) cnt = c2;
    }
    return cnt;
}

DEVI void m_team(const char* x, const char* __restrict__ w2, char* ws, int b, int P, bool typeA, int ii0, int fg0, int hj,
                 int tile64, int d0, int rot, unsigned yrot, int w, int lane, unsigned ooff, u32x4& xv) {
    const int n = lane & 15, g = lane >> 4, q = n >> 2, p = n & 3;
    const int r8 = lane >> 3, p16 = lane & 7;
    const unsigned voffw = r8 * 1024 + ((p16 ^ (2 * ((r8 >> 1) & 3))) * 16);
    const char* wb = w2 + d0 * 2 + (size_t)w * 32 * 1024;
    u64 epoch;
    m_phase_a(x, ws, wb, voffw, b, P, typeA, w, lane, ii0, fg0, hj, rot, epoch);

    const unsigned sdw = w * 4096;
    const unsigned abase = w * 4096 + (4 * g + q) * 128 + (((2 * g + (q >> 1)) & 3) * 32) + p * 8;
    u32x4 A[4][4];
    auto rdA = [&](int i) {
#pragma unroll
        for (int db = 0; db < 4; ++db) { const unsigned a = (abase ^ (db << 5)) + y_slot(i) * 16384; A[i & 3][db] = trread2(a, a + 2048); }
    };
    lds_wait_ge(SY_W2OK, 4);
    rdA(0); rdA(1); rdA(2); rdA(3);
    lds_wait_ge(SY_H47, 4);
    w2_step<4>(wb, voffw, sdw, yrot); w2_step<5>(wb, voffw, sdw, yrot); w2_step<6>(wb, voffw, sdw, yrot); w2_step<11>(wb, voffw, sdw, yrot);
#pragma unroll
    for (int i = 0; i < 4; ++i)
#pragma unroll
        for (int db = 0; db < 4; ++db) PIN4(A[i][db]);
    WAIT_LGKM0();
    w2_step<7>(wb, voffw, sdw, yrot); w2_step<8>(wb, voffw, sdw, yrot); w2_step<9>(wb, voffw, sdw, yrot); w2_step<10>(wb, voffw, sdw, yrot);
    xv = *(const GAS u32x4*)((const GAS char*)x + ooff);
    {
        const char* fb = ws + WS_FLAG + FL_H + (tile64 >> 1) * 128;
        const unsigned foff = (lane & 15) * 8, lo = (unsigned)epoch, hi = (unsigned)(epoch >> 32);
#pragma unroll 1
        for (unsigned s = 0; s < SPIN_CAP; ++s) {
            u32x2 f;
            asm volatile("global_load_dwordx2 %0, %1, %2 sc1\n\ts_waitcnt vmcnt(0)" : "=v"(f) : "v"(foff), "s"(fb) : "memory");
            if (__all(((f[0] ^ lo) | (f[1] ^ hi)) == 0u)) break;
            __builtin_amdgcn_s_sleep(2);
        }
        asm volatile("" ::: "memory");
    }
    STAMP(9); STAMP3(15);
    const char* hb = ws + WS_H + (size_t)(tile64 * 56 + w) * 4096;
    const unsigned voffh = lane * 16;
    u32x4 h[H_RING][4];
    auto issue_h = [&](int i) {
        const char* hp = hb + (size_t)(y_blk(i, yrot) * 16384u);
        GLOAD16(h[i % H_RING][0], voffh, hp, 0); GLOAD16(h[i % H_RING][1], voffh, hp, 1024);
        GLOAD16(h[i % H_RING][2], voffh, hp, 2048); GLOAD16(h[i % H_RING][3], voffh, hp, 3072);
    };
#pragma unroll
    for (int i = 0; i < H_RING; ++i) issue_h(i);
    f32x4 ay[4][4];
#pragma unroll
    for (int a = 0; a < 4; ++a)
#pragma unroll
        for (int t = 0; t < 4; ++t) ay[a][t] = f32x4{0, 0, 0, 0};
#define T1_WAIT(N, slot) asm volatile("s_waitcnt vmcnt(%4)" : "+v"(h[slot][0]), "+v"(h[slot][1]), "+v"(h[slot][2]), "+v"(h[slot][3]) : "n"(N) : "memory")
    auto ystep = [&](auto I) {
        constexpr int i = decltype(I)::v;
        T1_WAIT(y_after(i), i % H_RING);
        if (i == 0) STAMP(10);
        STAMP3(i);
#pragma unroll
        for (int db = 0; db < 4; ++db)
#pragma unroll
            for (int t = 0; t < 4; ++t) ay[db][t] = mfma(A[i & 3][db], h[i % H_RING][t], ay[db][t]);
        __builtin_amdgcn_sched_barrier(0);
        if (i + 4 < 14) rdA(i + 4);
        if (i == 2) (void)lds_arrive(SY_Y6, lane);
        if constexpr (i == 4 || i == 5) { WAIT_LGKM0(); w2_step<i + 8>(wb, voffw, sdw, yrot); }
        if (i + H_RING < 14) issue_h(i + H_RING);
        __builtin_amdgcn_sched_barrier(0);
    };
    ystep(IC<0>{}); ystep(IC<1>{}); ystep(IC<2>{}); ystep(IC<3>{}); ystep(IC<4>{}); ystep(IC<5>{}); ystep(IC<6>{});
    ystep(IC<7>{}); ystep(IC<8>{}); ystep(IC<9>{}); ystep(IC<10>{}); ystep(IC<11>{}); ystep(IC<12>{}); ystep(IC<13>{});
    STAMP(11); STAMP3(14);
#pragma unroll
    for (int t = 0; t < 4; ++t)
#pragma unroll
        for (int db = 0; db < 4; ++db) *(LAS f32x4*)((t * 4 + w) * 4096 + db * 1024 + lane * 16) = ay[db][t];
}

template <bool HAS_I>
DEVI void p2_unit(char* ws, const u32x4 (&Ur)[2][2], const u32x4 (&Ui)[2][2], const u32x4 (&Wf)[2], int zrow, int v, int lane, unsigned& sync_n,
                  const u32x4 (&C64)[2], const u32x4 (&S64)[2], const u32x4 (&N64)[2],
                  const u32x4 (&C32)[2], const u32x4 (&S32)[2], const u32x4 (&N32)[2],
                  const float (&tc)[8], const float (&tsn)[8]) {
    const int n = lane & 15, g = lane >> 4, q = n >> 2, p = n & 3;
    float br[8], bi[8];
#pragma unroll
    for (int mt = 0; mt < 2; ++mt) {
        f32x4 dr = {0, 0, 0, 0}, di = {0, 0, 0, 0};
#pragma unroll
        for (int ks = 0; ks < 2; ++ks) {
            dr = mfma(Ur[mt][ks], C64[ks], dr);
            di = mfma(Ur[mt][ks], N64[ks], di);
            if (HAS_I) { dr = mfma(Ui[mt][ks], S64[ks], dr); di = mfma(Ui[mt][ks], C64[ks], di); }
        }
#pragma unroll
        for (int r = 0; r < 4; ++r) {
            const float c = tc[4 * mt + r], s = tsn[4 * mt + r];
            br[4 * mt + r] = dr[r] * c + di[r] * s;
            bi[4 * mt + r] = di[r] * c - dr[r] * s;
        }
    }
    u32x4 Br = {pk2(br[0], br[1]), pk2(br[2], br[3]), pk2(br[4], br[5]), pk2(br[6], br[7])};
    u32x4 Bi = {pk2(bi[0], bi[1]), pk2(bi[2], bi[3]), pk2(bi[4], bi[5]), pk2(bi[6], bi[7])};
    float yr[8], yi[8];
#pragma unroll
    for (int t = 0; t < 2; ++t) {
        f32x4 dr = {0, 0, 0, 0}, di = {0, 0, 0, 0};
        dr = mfma(C32[t], Br, dr); dr = mfma(S32[t], Bi, dr);
        di = mfma(C32[t], Bi, di); di = mfma(N32[t], Br, di);
#pragma unroll
        for (int r = 0; r < 4; ++r) {
            const float wre = bf_lo(Wf[t][r]), wim = bf_hi(Wf[t][r]);
            yr[4 * t + r] = dr[r] * wre - di[r] * wim;
            yi[4 * t + r] = dr[r] * wim + di[r] * wre;
        }
    }
    u32x4 Yr = {pk2(yr[0], yr[1]), pk2(yr[2], yr[3]), pk2(yr[4], yr[5]), pk2(yr[6], yr[7])};
    u32x4 Yi = {pk2(yi[0], yi[1]), pk2(yi[2], yi[3]), pk2(yi[4], yi[5]), pk2(yi[6], yi[7])};
#pragma unroll
    for (int mt = 0; mt < 2; ++mt) {
        f32x4 dr = {0, 0, 0, 0}, di = {0, 0, 0, 0};
        dr = mfma(C32[mt], Yr, dr); dr = mfma(N32[mt], Yi, dr);
        di = mfma(C32[mt], Yi, di); di = mfma(S32[mt], Yr, di);
        float pr[4], pi[4];
#pragma unroll
        for (int r = 0; r < 4; ++r) {
            const float c = tc[4 * mt + r] * (1.0f / 2048.0f), s = tsn[4 * mt + r] * (1.0f / 2048.0f);
            pr[r] = dr[r] * c - di[r] * s;
            pi[r] = di[r] * c + dr[r] * s;
        }
        const unsigned a = L_BP + (16 * v + n) * 72 + (16 * mt + 4 * g) * 2;
        lds_wr8(a, u32x2{pk2(pr[0], pr[1]), pk2(pr[2], pr[3])});
        lds_wr8(a + 64 * 72, u32x2{pk2(pi[0], pi[1]), pk2(pi[2], pi[3])});
    }
    sync_n += 4; team_sync(SY_TEAM, sync_n, lane);
#pragma unroll
    for (int nt = 0; nt < 2; ++nt) {
        f32x4 dr = {0, 0, 0, 0}, di = {0, 0, 0, 0};
#pragma unroll
        for (int ks = 0; ks < 2; ++ks) {
            const unsigned a = L_BP + (32 * ks + 8 * g + q) * 72 + nt * 32 + p * 8;
            const u32x4 pr = trread2(a, a + 4 * 72), pi = trread2(a + 64 * 72, a + 64 * 72 + 4 * 72);
            dr = mfma(C64[ks], pr, dr); dr = mfma(N64[ks], pi, dr);
            if (HAS_I) { di = mfma(C64[ks], pi, di); di = mfma(S64[ks], pr, di); }
        }
        const unsigned a = L_ZS + (16 * nt + n) * 144 + (16 * v + 4 * g) * 2;
        lds_wr8(a, u32x2{pk2(dr[0], dr[1]), pk2(dr[2], dr[3])});
        if (HAS_I) lds_wr8(a + 32 * 144, u32x2{pk2(di[0], di[1]), pk2(di[2], di[3])});
    }
    sync_n += 4; team_sync(SY_TEAM, sync_n, lane);
    {
        const __amdgpu_buffer_rsrc_t rs = rsrc(ws + WS_ZT, 512 * 4096);
#pragma unroll
        for (int it = 0; it < (HAS_I ? 2 : 1); ++it) {
            const int row = 8 * v + (lane >> 3), ch = lane & 7;
            const u32x4 o = lds_rd16(L_ZS + (it * 32 + row) * 144 + ch * 16);
            st_sc1(o, rs, (zrow + it) * 4096 + row * 128 + ch * 16);
        }
    }
}

#define GLOAD_U16(dst, voff, sbase, imm) asm volatile("global_load_ushort %0, %1, %2 offset:%3" : "=v"(dst) : "v"(voff), "s"(sbase), "n"(imm) : "memory")
DEVI void f_gen(int fg, unsigned foff, int rot, int u, int lane) {
    const int n = lane & 15, g = lane >> 4;
    const int c = 16 * fg + n;
    const int k2 = (c == 1) ? 256 : (c >> 1), part = (c == 1) ? 0 : (c & 1);
    const float st = (float)k2 * (1.0f / 512.0f), ph = (float)part * 0.25f;
    const unsigned sg = (k2 & 1) ? 0x80008000u : 0u;
    const float d0 = (float)(64 * u + 8 * g + 64 * rot);
#pragma unroll
    for (int e = 0; e < 2; ++e) {
        const float x0 = __builtin_amdgcn_fractf(st * (d0 + 32.0f * e) + ph);
        float v[8];
#pragma unroll
        for (int j = 0; j < 8; ++j) v[j] = cosr(x0 + (float)j * st);
        const u32x4 o = {pk2(v[0], v[1]), pk2(v[2], v[3]), pk2(v[4], v[5]), pk2(v[6], v[7])};
        const u32x4 o2 = {o[0] ^ sg, o[1] ^ sg, o[2] ^ sg, o[3] ^ sg};
        lds_wr16(foff + (2 * u + e) * 1024 + lane * 16, o);
        lds_wr16(foff + (2 * u + e + 8) * 1024 + lane * 16, o2);
    }
}
DEVI void s_team(const char* x, const char* __restrict__ w1, const char* __restrict__ w2, const unsigned short* wr, const unsigned short* wi, char* ws, int b,
                 bool typeA, int col0, int fg0, int tile64, int d0, int rot, unsigned yrot, int u, int lane, unsigned ooff, u32x4& xv) {
    const int n = lane & 15, g = lane >> 4, q = n >> 2, p = n & 3;
    const u64 ereq = epoch_request(ws);
    const char* wb = w1 + col0 * 2;
    const int r8 = lane >> 3, p16 = lane & 7;
    const unsigned v128 = r8 * 3584 + (p16 & 1) * 16;
    unsigned va[2];
    if (typeA) {
#pragma unroll
        for (int j = 0; j < 2; ++j) va[j] = v128 + (((p16 >> 1) ^ (((r8 >> 1) & 1) | (j << 1))) * 32) + (2 * u + j) * 8 * 3584;
    } else {
        const int r16 = lane >> 2, c4 = lane & 3;
        va[0] = va[1] = r16 * 3584 + (((c4 >> 1) ^ (r16 >> 3)) * 32) + (c4 & 1) * 16 + u * 16 * 3584;
    }
    const unsigned sdA = typeA ? u * 2048 : u * 1024;
    auto stage_a = [&](auto S) {
        constexpr int s = decltype(S)::v;
        const char* sb = wb + (size_t)(((s + rot) & 7) * (64 * 3584));
        if (typeA) { dmaq<s * 8192>(va[0], sb, sdA); dmaq<s * 8192 + 1024>(va[1], sb, sdA); }
        else dmaq<s * 4096>(va[0], sb, sdA);
    };
    stage_a(IC<0>{}); stage_a(IC<1>{});
    const int rs = b & 63, cq = b >> 6;
    const int jv = rs & 3, ja = (rs >> 2) & 1, jt = (rs >> 3) & 1, jg = rs >> 4;
    unsigned wv[2][4][2], wsp;
    {
        const unsigned k0 = 16 * jv + 8 * ja + 2 * u + 64 * (16 * jt + 4 * jg);
        const unsigned off0 = (k0 * 257 + 64 * cq + lane) * 2;
#pragma unroll
        for (int r = 0; r < 4; ++r) {
            const unsigned off = off0 + r * (64 * 514);
            GLOAD_U16(wv[0][r][0], off, wr, 0); GLOAD_U16(wv[0][r][1], off, wi, 0);
            GLOAD_U16(wv[1][r][0], off, wr, 514); GLOAD_U16(wv[1][r][1], off, wi, 514);
        }
        const int unit = 2 * b + ((lane >> 3) & 1), reg = (lane >> 1) & 3;
        const unsigned k1 = 16 * (unit >> 7) + (unit & 15) + 64 * (16 * ((unit >> 6) & 1) + 4 * ((unit >> 4) & 3) + reg);
        const unsigned long long a = (unsigned long long)((lane & 1) ? wi : wr) + (k1 * 257 + 256) * 2;
        asm volatile("global_load_ushort %0, %1, off" : "=v"(wsp) : "v"(a) : "memory");
    }
    stage_a(IC<2>{});
    STAMP(1);
    f_gen(fg0, L_F0, rot, u, lane);
    if (!typeA) { f_gen(fg0 + 1, L_F1, rot, u, lane); f_gen(fg0 + 2, L_F1 + 16384, rot, u, lane); }
    STAMP(2);
    const char* wb2 = wb + (typeA ? 128 : 64);
    const unsigned vbb = v128 + (((p16 >> 1) ^ (((r8 >> 1) & 1) | ((u & 1) << 1))) * 32) + u * 8 * 3584;
    const unsigned sdB = L_W1B + u * 1024;
    auto stage_b = [&](auto K) {
        constexpr int k = decltype(K)::v;
        dmaq<k * 4096>(vbb, wb2 + (size_t)(((k + 2 * rot) & 15) * (32 * 3584)), sdB);
    };
    const u32x4 ones = {0x3f803f80u, 0x3f803f80u, 0x3f803f80u, 0x3f803f80u};
    {
        const unsigned swz = (q >> 1) | ((g & 1) << 1);
        const unsigned abase = typeA ? (((8 * g + q) * 128 + swz * 32 + p * 8) ^ (u << 5)) : ((8 * g + q) * 64 + (((u ^ g) & 1) * 32) + p * 8);
        const unsigned kpitch = typeA ? 4096 : 2048, hpitch = typeA ? 512 : 256;
        f32x4 cs = {0, 0, 0, 0};
        auto stage = [&](auto S) {
            constexpr int s = decltype(S)::v;
            STAMP2(s);
            if (typeA) {
                if (s < 2) WAIT_VM(21); else if (s < 5) WAIT_VM(4); else if (s == 5) WAIT_VM(6); else if (s == 6) WAIT_VM(12); else WAIT_VM(18);
            } else {
                if (s < 2) WAIT_VM(19); else if (s < 5) WAIT_VM(2); else if (s == 5) WAIT_VM(4); else if (s == 6) WAIT_VM(11); else WAIT_VM(18);
            }
            WAIT_LGKM0();
            STAMP2(8 + s);
            barrier();
            if (s == 0) STAMP(3);
            const unsigned a0 = abase + (2 * s) * kpitch, a1 = a0 + kpitch;
            const u32x4 c0 = trread2(a0, a0 + hpitch), c1 = trread2(a1, a1 + hpitch);
            if constexpr (s + 3 < 8) stage_a(IC<s + 3>{});
            if constexpr (s == 2) {
                asm volatile("" : "+v"(wv[0][0][0]), "+v"(wv[0][0][1]), "+v"(wv[0][1][0]), "+v"(wv[0][1][1]), "+v"(wv[0][2][0]), "+v"(wv[0][2][1]), "+v"(wv[0][3][0]), "+v"(wv[0][3][1]),
                                  "+v"(wv[1][0][0]), "+v"(wv[1][0][1]), "+v"(wv[1][1][0]), "+v"(wv[1][1][1]), "+v"(wv[1][2][0]), "+v"(wv[1][2][1]), "+v"(wv[1][3][0]), "+v"(wv[1][3][1]), "+v"(wsp));
#pragma unroll
                for (int e = 0; e < 2; ++e) {
                    u32x4 o;
#pragma unroll
                    for (int r = 0; r < 4; ++r) o[r] = wv[e][r][0] | (wv[e][r][1] << 16);
                    lds_wr16(L_WS + lane * 144 + (2 * u + e) * 16, o);
                }
            }
            if constexpr (s == 4) {
                const __amdgpu_buffer_rsrc_t rw = rsrc(ws + WS_WT, 257 * 8192);
#pragma unroll
                for (int e = 0; e < 2; ++e) {
                    const int k2l = 16 * u + 8 * e + (lane >> 3);
                    const u32x4 o = lds_rd16(L_WS + k2l * 144 + (lane & 7) * 16);
                    st_sc1(o, rw, (64 * cq + k2l) * 8192 + ((jv * 2 + jt) * 64 + 16 * jg + 8 * ja + (lane & 7)) * 16);
                }
                if (u == 0 && lane < 16) __builtin_amdgcn_raw_buffer_store_b16((unsigned short)wsp, rw, 256 * 8192 + 2 * b * 16 + lane * 2, 0, 16);
            }
            if constexpr (s == 5 || s == 6) {
                stage_b(IC<8 * (s - 5)>{}); stage_b(IC<8 * (s - 5) + 1>{}); stage_b(IC<8 * (s - 5) + 2>{}); stage_b(IC<8 * (s - 5) + 3>{});
                stage_b(IC<8 * (s - 5) + 4>{}); stage_b(IC<8 * (s - 5) + 5>{}); stage_b(IC<8 * (s - 5) + 6>{}); stage_b(IC<8 * (s - 5) + 7>{});
            }
            cs = mfma(c0, ones, cs);
            cs = mfma(c1, ones, cs);
        };
        stage(IC<0>{}); stage(IC<1>{}); stage(IC<2>{}); stage(IC<3>{}); stage(IC<4>{}); stage(IC<5>{}); stage(IC<6>{}); stage(IC<7>{});
        if (n == 0 && (typeA || u < 2)) *(LAS f32x4*)(L_CS + (16 * u + 4 * g) * 4) = cs;
    }
    WAIT_LGKM0();
    barrier();
    STAMP(4);
    {
        const unsigned voffw = r8 * 1024 + ((p16 ^ (2 * ((r8 >> 1) & 3))) * 16);
        const char* wb3 = w2 + d0 * 2 + (size_t)u * 32 * 1024;
        const unsigned sd = u * 4096;
        w2_step<0>(wb3, voffw, sd, yrot); w2_step<1>(wb3, voffw, sd, yrot); w2_step<2>(wb3, voffw, sd, yrot); w2_step<3>(wb3, voffw, sd, yrot);
    }
    WAIT_VM(16);
    const u64 epoch = epoch_finish(ereq);
    (void)lds_arrive(SY_BOK, lane);
    if (lds_arrive(SY_AARR, lane) == 7) post_replicas(ws, FL_A, b, lane, epoch);
    lds_wait_ge(SY_BOK, 4);
    STAMP(5);
    {
        f32x4 cs = {0, 0, 0, 0};
        const unsigned swz = (q >> 1) | ((g & 1) << 1);
        const unsigned abase = L_W1B + (((8 * g + q) * 128 + swz * 32 + p * 8) ^ (u << 5));
#pragma unroll
        for (int ks = 0; ks < 16; ++ks) { const unsigned a = abase + ks * 4096; cs = mfma(trread2(a, a + 512), ones, cs); }
        if (n == 0) *(LAS f32x4*)(L_CS + (64 + 16 * u + 4 * g) * 4) = cs;
    }
    WAIT_LGKM0();
    (void)lds_arrive(SY_CS2, lane);
    u32x4 C64[2], S64[2], N64[2], C32[2], S32[2], N32[2];
    float tc[8], tsn[8];
    {
        const int m = 16 * u + n;
        const float mf = (float)m;
        {
            const float st = mf * (1.0f / 64.0f), a0 = __builtin_amdgcn_fractf(st * (float)(8 * g));
            float c[8], sn[8];
#pragma unroll
            for (int j = 0; j < 8; ++j) { const float a = a0 + (float)j * st; c[j] = cosr(a); sn[j] = sinr(a); }
            const unsigned sg = (m & 1) ? 0x80008000u : 0u;
#pragma unroll
            for (int e = 0; e < 4; ++e) {
                C64[0][e] = pk2(c[2 * e], c[2 * e + 1]); S64[0][e] = pk2(sn[2 * e], sn[2 * e + 1]); N64[0][e] = S64[0][e] ^ 0x80008000u;
                C64[1][e] = C64[0][e] ^ sg; S64[1][e] = S64[0][e] ^ sg; N64[1][e] = N64[0][e] ^ sg;
            }
        }
        {
            const float st = (float)n * (1.0f / 32.0f), a0 = st * (float)(4 * g);
            float c[4], sn[4];
#pragma unroll
            for (int j = 0; j < 4; ++j) { const float a = a0 + (float)j * st; c[j] = cosr(a); sn[j] = sinr(a); }
            const unsigned sg = (n & 1) ? 0x80008000u : 0u;
#pragma unroll
            for (int e = 0; e < 2; ++e) {
                C32[0][e] = pk2(c[2 * e], c[2 * e + 1]); S32[0][e] = pk2(sn[2 * e], sn[2 * e + 1]);
                C32[0][e + 2] = C32[0][e] ^ sg; S32[0][e + 2] = S32[0][e] ^ sg;
            }
#pragma unroll
            for (int e = 0; e < 4; ++e) {
                C32[1][e] = C32[0][e] ^ 0x80000000u; S32[1][e] = S32[0][e] ^ 0x80000000u;
                N32[0][e] = S32[0][e] ^ 0x80008000u; N32[1][e] = S32[0][e] ^ 0x00008000u;
            }
        }
        {
            const float st = mf * (1.0f / 2048.0f), a0 = st * (float)(4 * g);
#pragma unroll
            for (int j = 0; j < 8; ++j) { const float a = a0 + (float)(16 * (j >> 2) + (j & 3)) * st; tc[j] = cosr(a); tsn[j] = sinr(a); }
        }
#pragma unroll
        for (int e = 0; e < 2; ++e) { PIN4(C64[e]); PIN4(S64[e]); PIN4(N64[e]); PIN4(C32[e]); PIN4(S32[e]); PIN4(N32[e]); }
#pragma unroll
        for (int j = 0; j < 8; ++j) { PIN4(tc[j]); PIN4(tsn[j]); }
    }
    WAIT_VM(0);
    (void)lds_arrive(SY_W2OK, lane);
    STAMP(6);
    __builtin_amdgcn_s_setprio(1);

    const int v = u;
    if (v == 0) {
        poll256(ws, FL_A, b, lane, epoch);
        if (lane == 0) __hip_atomic_store((LAS unsigned*)SY_AOK, 1u, __ATOMIC_RELAXED, __HIP_MEMORY_SCOPE_WORKGROUP);
    } else lds_wait_ge(SY_AOK, 1u);
    STAMP(7);
    u32x4 Ur[2][2], Ui[2][2], Wf[2], Wg[2];
    {
        const __amdgpu_buffer_rsrc_t rx = rsrc(ws + WS_X1T, 512 * 4096), rw = rsrc(ws + WS_WT, 257 * 8192);
#pragma unroll
        for (int mt = 0; mt < 2; ++mt)
#pragma unroll
            for (int ks = 0; ks < 2; ++ks) {
                const unsigned o = b * 8192 + (2 * (16 * mt + n) + ks) * 128 + g * 16;
                Ur[mt][ks] = ld_sc1(rx, o);
                Ui[mt][ks] = ld_sc1(rx, o + 64);
            }
#pragma unroll
        for (int t = 0; t < 2; ++t) {
            Wf[t] = ld_sc1(rw, b * 8192 + ((v * 2 + t) * 64 + lane) * 16);
            if (b == 0) Wg[t] = ld_sc1(rw, 256 * 8192 + ((v * 2 + t) * 64 + lane) * 16);
            else Wg[t] = u32x4{0, 0, 0, 0};
        }
    }
    unsigned sync_n = 0;
    if (b != 0) p2_unit<true>(ws, Ur, Ui, Wf, 2 * b, v, lane, sync_n, C64, S64, N64, C32, S32, N32, tc, tsn);
    else {
        p2_unit<false>(ws, Ur, Ur, Wf, 0, v, lane, sync_n, C64, S64, N64, C32, S32, N32, tc, tsn);
        p2_unit<false>(ws, Ui, Ui, Wg, 1, v, lane, sync_n, C64, S64, N64, C32, S32, N32, tc, tsn);
    }
    STAMP(8);
    WAIT_VM(0);
    if (lds_arrive(SY_ZARR, lane) == 3) post_replicas(ws, FL_Z, b, lane, epoch);
    STAMP(9); STAMP3(9);
    xv = *(const GAS u32x4*)((const GAS char*)x + ooff);
    u32x4 G[4][4];
#pragma unroll
    for (int db = 0; db < 4; ++db) {
        const int d = d0 + 16 * db + n;
        float c[4], s[4];
#pragma unroll
        for (int jj = 0; jj < 4; ++jj) {
            const float a = (float)(((64 * v + 4 * g + jj) * d) & 511) * (1.0f / 512.0f);
            c[jj] = cosr(a); s[jj] = sinr(a);
        }
        const float ar = (float)((16 * d) & 511) * (1.0f / 512.0f);
        const float cr = cosr(ar), sr = sinr(ar);
#pragma unroll
        for (int ks = 0; ks < 4; ++ks) {
            u32x4 o;
#pragma unroll
            for (int jj = 0; jj < 4; ++jj) o[jj] = pk2(c[jj], -s[jj]);
            if (ks == 0 && v == 0 && g == 0) o[0] = pk2(0.5f, (d & 1) ? -0.5f : 0.5f);
            G[db][ks] = o;
            PIN4(G[db][ks]);
            if (ks < 3) {
#pragma unroll
                for (int jj = 0; jj < 4; ++jj) {
                    const float c2 = c[jj] * cr - s[jj] * sr, s2 = s[jj] * cr + c[jj] * sr;
                    c[jj] = c2; s[jj] = s2;
                }
            }
        }
    }
    STAMP(10); STAMP3(10);
    if (v == 0) {
        poll256(ws, FL_Z, b, lane, epoch);
        if (lane == 0) {
            __hip_atomic_store((LAS unsigned*)SY_ZOK, 1u, __ATOMIC_RELAXED, __HIP_MEMORY_SCOPE_WORKGROUP);
            if (b == 0) __hip_atomic_store((GAS u64*)(ws + WS_FLAG), epoch, __ATOMIC_RELAXED, __HIP_MEMORY_SCOPE_AGENT);
        }
    } else lds_wait_ge(SY_ZOK, 1u);
    lds_wait_ge(SY_Y6, 4);
    STAMP(11); STAMP3(11);
    const unsigned zreg = L_Z + v * 16384;
    {
        const __amdgpu_buffer_rsrc_t rz = rsrc(ws + WS_ZT, 512 * 4096);
        const int r8 = lane >> 3, ch = lane & 7;
        u32x4 z[16];
#pragma unroll
        for (int i = 0; i < 16; ++i) z[i] = ld_sc1(rz, (128 * v + 8 * i + r8) * 4096 + tile64 * 128 + ch * 16);
#pragma unroll
        for (int i = 0; i < 16; ++i) lds_wr16(zreg + (8 * i + r8) * 128 + ((ch ^ (4 * ((r8 >> 1) & 1))) * 16), z[i]);
    }
    STAMP(12); STAMP3(12);
    f32x4 acc[4][4];
#pragma unroll
    for (int a = 0; a < 4; ++a)
#pragma unroll
        for (int t = 0; t < 4; ++t) acc[a][t] = f32x4{0, 0, 0, 0};
#pragma unroll
    for (int ks = 0; ks < 4; ++ks)
#pragma unroll
        for (int t = 0; t < 4; ++t) {
            const unsigned a = zreg + (32 * ks + 8 * g + q) * 128 + (((t >> 1) ^ ((q >> 1) & 1)) * 64) + p * 16 + (t & 1) * 8;
            const u32x4 z = trread2(a, a + 512);
#pragma unroll
            for (int db = 0; db < 4; ++db) acc[db][t] = mfma(G[db][ks], z, acc[db][t]);
        }
#pragma unroll
    for (int t = 0; t < 4; ++t)
#pragma unroll
        for (int db = 0; db < 4; ++db) *(LAS f32x4*)(zreg + (t * 4 + db) * 1024 + lane * 16) = acc[db][t];
    STAMP(13); STAMP3(13);
}

__global__ void __launch_bounds__(512, 2) fused_kernel(const char* x, const unsigned short* wr, const unsigned short* wi,
                                                        const char* __restrict__ w1, const char* __restrict__ w2, char* ws,
                                                        char* __restrict__ out) {
    const int b = blockIdx.x, lane = threadIdx.x & 63, w = __builtin_amdgcn_readfirstlane(threadIdx.x >> 6);
    STAMP(0);
    if (w >= 4) STAMP3(0);
    const int xcd = b & 7, slot = b >> 3;
    if (w == 7 || w == 3) {
        unsigned long long pc;
        asm volatile("s_getpc_b64 %0" : "=s"(pc));
        const unsigned piece = (unsigned)(2 * slot + (w >> 2)) % CODE_PIECES;
        const unsigned voff = piece * 4096 + lane * 64;
        asm volatile("s_mov_b32 m0, %2\n\ts_nop 0\n\tglobal_load_lds_dword %0, %1" :: "v"(voff), "s"(pc), "s"(L_CS) : "memory");
    }
    const int hf = xcd & 1, P = 4 * (xcd >> 1) + (slot >> 3), i = slot & 7;
    const bool typeA = i < 4;
    const int col0 = 896 * hf + (typeA ? 128 * i : 512 + 96 * (i - 4));
    const int fg0 = 16 * hf + (typeA ? i : 4 + 3 * (i - 4));
    const int rot = (i + 2 * (slot >> 3)) & 7;
    const int tile64 = 4 * xcd + (slot >> 3), d0 = 64 * (slot & 7);
    const unsigned yrot = (unsigned)(((7 * (slot & 7)) >> 2) + 3 * (slot >> 3)) % 14u;
    if (threadIdx.x < 16) *(LAS unsigned*)(unsigned)(L_SY + threadIdx.x * 4) = 0u;
    const int n = lane & 15, g = lane >> 4;
    const int T = w & 3, dp = w >> 2;
    const unsigned tok = 32u * (32 * (T >> 1) + 8 * (n >> 2) + 4 * (T & 1) + (n & 3)) + tile64;
    const unsigned ooff = tok * 1024u + (d0 + 32 * dp + 8 * g) * 2;
    u32x4 xv;
    if (w < 4) m_team(x, w2, ws, b, P, typeA, col0 >> 5, fg0, 8 * hf + i, tile64, d0, rot, yrot, w, lane, ooff, xv);
    else s_team(x, w1, w2, wr, wi, ws, b, typeA, col0, fg0, tile64, d0, rot, yrot, w - 4, lane, ooff, xv);
    WAIT_LGKM0();
    barrier();
    STAMP(14);
    float y[8], s[8];
#pragma unroll
    for (int j = 0; j < 8; ++j) { y[j] = 0.0f; s[j] = 0.0f; }
#pragma unroll
    for (int e = 0; e < 2; ++e) {
        const unsigned a = (2 * dp + (g >> 1)) * 1024 + (n + 16 * (2 * (g & 1) + e)) * 16;
#pragma unroll
        for (int k = 0; k < 4; ++k) {
            const f32x4 py = *(LAS f32x4*)(a + (T * 4 + k) * 4096), ps = *(LAS f32x4*)(a + L_Z + k * 16384 + T * 4096);
#pragma unroll
            for (int r = 0; r < 4; ++r) { y[4 * e + r] += py[r]; s[4 * e + r] += ps[r]; }
        }
    }
    float o[8];
#pragma unroll
    for (int j = 0; j < 8; ++j) {
        const float yb = bf_round(y[j]);
        const float gg = bf_round(yb / (1.0f + __expf(-yb)));
        const float sb = bf_round(s[j] * (2.0f / 512.0f));
        const float xx = (j & 1) ? bf_hi(xv[j >> 1]) : bf_lo(xv[j >> 1]);
        o[j] = xx + bf_round(sb * gg);
    }
    const u32x4 ov = {pk2(o[0], o[1]), pk2(o[2], o[3]), pk2(o[4], o[5]), pk2(o[6], o[7])};
    *(GAS u32x4*)((GAS char*)out + ooff) = ov;
    STAMP(15);
}

extern "C" void kernel_launch(void* const* d_in, const int* in_sizes, int n_in, void* d_out, int out_size, void* d_ws,
                              size_t ws_size, hipStream_t stream) {
    (void)in_sizes; (void)n_in; (void)out_size; (void)ws_size;
    (void)hipFuncSetAttribute((const void*)fused_kernel, hipFuncAttributeMaxDynamicSharedMemorySize, L_END);
    fused_kernel<<<256, 512, L_END, stream>>>((const char*)d_in[0], (const unsigned short*)d_in[1], (const unsigned short*)d_in[2],
                                              (const char*)d_in[4], (const char*)d_in[5], (char*)d_ws, (char*)d_out);
}
```

```cpp
#include <hip/hip_runtime.h>
#include <stdint.h>
#pragma clang diagnostic ignored "-Wint-to-pointer-cast"

typedef short s16x4 __attribute__((ext_vector_type(4)));
typedef __bf16 bf16x8 __attribute__((ext_vector_type(8)));
typedef __bf16 bf16x2 __attribute__((ext_vector_type(2)));
typedef float f32x4 __attribute__((ext_vector_type(4)));
typedef float f32x2 __attribute__((ext_vector_type(2)));
typedef unsigned u32x4 __attribute__((ext_vector_type(4)));
typedef unsigned u32x2 __attribute__((ext_vector_type(2)));
typedef unsigned long long u64;
#define LAS __attribute__((address_space(3)))
#define GAS __attribute__((address_space(1)))
#define DEVI __device__ __forceinline__

constexpr size_t WS_H = 0;
constexpr size_t WS_X1T = 8u << 20;
constexpr size_t WS_WT = 12u << 20;
constexpr size_t WS_ZT = 16u << 20;
constexpr size_t WS_FLAG = 20u << 20;
constexpr size_t WS_TS = 24u << 20;
constexpr unsigned FL_H = 4096, FL_A = 8192, FL_Z = 8192 + 16384;

constexpr unsigned L_W1B = 65536, L_F0 = 131072, L_F1 = 32768;
constexpr unsigned L_CS = 161792, L_ST = L_CS + 512, L_SY = L_ST + 1024, L_END = L_SY + 64;
constexpr unsigned L_Z = 81920, L_BP = 131072, L_ZS = L_BP + 18432, L_WS = 147456;
constexpr unsigned SY_TEAM = L_SY, SY_ZARR = L_SY + 4, SY_ZOK = L_SY + 8, SY_AARR = L_SY + 12, SY_AOK = L_SY + 16, SY_HARR = L_SY + 20,
                   SY_BOK = L_SY + 24, SY_CS2 = L_SY + 28, SY_H47 = L_SY + 32, SY_W2OK = L_SY + 36, SY_Y6 = L_SY + 40, SY_XR = L_SY + 44;
constexpr unsigned SPIN_CAP = 1u << 16;
constexpr unsigned CODE_PIECES = 11;

#define STAMPC(id) do { } while (0)
#define STAMP4(a, b) do { } while (0)
#define STAMP2(id) do { } while (0)
#define STAMP3(id) do { } while (0)
#define STAMP(id) do { } while (0)

DEVI unsigned pk2(float a, float b) {
    f32x2 f = {a, b};
    return __builtin_bit_cast(unsigned, __builtin_convertvector(f, bf16x2));
}
DEVI float bf_round(float a) { return __builtin_bit_cast(float, pk2(a, a) << 16); }
DEVI float bf_lo(unsigned u) { return __builtin_bit_cast(float, u << 16); }
DEVI float bf_hi(unsigned u) { return __builtin_bit_cast(float, u & 0xffff0000u); }
DEVI f32x4 mfma(u32x4 a, u32x4 b, f32x4 c) {
    return __builtin_amdgcn_mfma_f32_16x16x32_bf16(__builtin_bit_cast(bf16x8, a), __builtin_bit_cast(bf16x8, b), c, 0, 0, 0);
}
DEVI void mfma_ip(f32x4& c, u32x4 a, u32x4 b, bool first) {
    if (first) asm volatile("v_mfma_f32_16x16x32_bf16 %0, %1, %2, 0" : "=v"(c) : "v"(a), "v"(b));
    else asm volatile("v_mfma_f32_16x16x32_bf16 %0, %1, %2, %0" : "+v"(c) : "v"(a), "v"(b));
}
#define MFMA_DONE() asm volatile("s_nop 7\n\ts_nop 1" ::: "memory")
#define WAIT_LGKM(n) __builtin_amdgcn_s_waitcnt(0xC07F | ((n) << 8))
DEVI u32x2 trread(unsigned addr) {
    s16x4 v = __builtin_amdgcn_ds_read_tr16_b64_v4i16((LAS s16x4*)addr);
    return __builtin_bit_cast(u32x2, v);
}
DEVI u32x4 trread2(unsigned a0, unsigned a1) {
    u32x2 lo = trread(a0), hi = trread(a1);
    u32x4 r = {lo[0], lo[1], hi[0], hi[1]};
    return r;
}
DEVI u32x4 lds_rd16(unsigned addr) { return *(LAS u32x4*)addr; }
DEVI void lds_wr16(unsigned addr, u32x4 v) { *(LAS u32x4*)addr = v; }
DEVI void lds_wr8(unsigned addr, u32x2 v) { *(LAS u32x2*)addr = v; }
DEVI float cosr(float rev) { return __builtin_amdgcn_cosf(rev); }
DEVI float sinr(float rev) { return __builtin_amdgcn_sinf(rev); }
DEVI void barrier() { asm volatile("s_barrier" ::: "memory"); }
#define WAIT_VM(n) asm volatile("s_waitcnt vmcnt(%0)" ::"n"(n) : "memory")
#define WAIT_LGKM0() asm volatile("s_waitcnt lgkmcnt(0)" ::: "memory")
#define PIN4(a) asm volatile("" : "+v"(a))
DEVI __amdgpu_buffer_rsrc_t rsrc(char* base, unsigned bytes) { return __builtin_amdgcn_make_buffer_rsrc(base, 0, bytes, 0x00020000); }
DEVI void st_sc1(u32x4 v, __amdgpu_buffer_rsrc_t r, unsigned off) { __builtin_amdgcn_raw_buffer_store_b128(v, r, off, 0, 16); }
DEVI u32x4 ld_sc1(__amdgpu_buffer_rsrc_t r, unsigned off) { return __builtin_amdgcn_raw_buffer_load_b128(r, off, 0, 16); }

DEVI void dma16(unsigned voff, const void* sbase, unsigned dst) {
    unsigned keep;
    asm volatile("s_mov_b32 %0, m0\n\ts_mov_b32 m0, %3\n\ts_nop 0\n\tglobal_load_lds_dwordx4 %1, %2\n\ts_mov_b32 m0, %0"
                 : "=&s"(keep) : "v"(voff), "s"(sbase), "s"(dst) : "memory");
}
template <unsigned IMM>
DEVI void dmaq(unsigned voff, const void* sbase, unsigned sdst) {
    asm volatile("s_add_u32 m0, %2, %3\n\ts_nop 0\n\tglobal_load_lds_dwordx4 %0, %1" :: "v"(voff), "s"(sbase), "s"(sdst), "n"(IMM) : "memory", "scc");
}
DEVI unsigned lds_ld_relaxed(unsigned addr) { return __hip_atomic_load((LAS unsigned*)addr, __ATOMIC_RELAXED, __HIP_MEMORY_SCOPE_WORKGROUP); }
DEVI unsigned lds_arrive(unsigned cnt, int lane) {
    unsigned old = 0;
    if (lane == 0) old = __hip_atomic_fetch_add((LAS unsigned*)cnt, 1u, __ATOMIC_RELAXED, __HIP_MEMORY_SCOPE_WORKGROUP);
    return __builtin_amdgcn_readfirstlane(old);
}
DEVI void lds_wait_ge(unsigned cnt, unsigned target) {
#pragma unroll 1
    for (unsigned s = 0; s < SPIN_CAP * 16; ++s) {
        asm volatile("" ::: "memory");
        if ((unsigned)__builtin_amdgcn_readfirstlane(lds_ld_relaxed(cnt)) >= target) break;
        __builtin_amdgcn_s_sleep(1);
    }
    asm volatile("" ::: "memory");
}
DEVI void team_sync(unsigned cnt, unsigned target, int lane) {
    WAIT_LGKM0();
    if (lane == 0) __hip_atomic_fetch_add((LAS unsigned*)cnt, 1u, __ATOMIC_RELAXED, __HIP_MEMORY_SCOPE_WORKGROUP);
    lds_wait_ge(cnt, target);
}
DEVI u64 epoch_request(const char* ws) { return __hip_atomic_load((const GAS u64*)(ws + WS_FLAG), __ATOMIC_RELAXED, __HIP_MEMORY_SCOPE_AGENT); }
DEVI u64 epoch_finish(u64 e) {
    const u64 v = ((u64)(unsigned)__builtin_amdgcn_readfirstlane((unsigned)(e >> 32)) << 32) | (unsigned)__builtin_amdgcn_readfirstlane((unsigned)e);
    return v + 1;
}
DEVI void post_replicas(char* ws, unsigned off, int b, int lane, u64 epoch) {
    if (lane < 8) __hip_atomic_store((GAS u64*)(ws + WS_FLAG + off + lane * 2048 + b * 8), epoch, __ATOMIC_RELAXED, __HIP_MEMORY_SCOPE_AGENT);
}
DEVI void poll256(const char* ws, unsigned off, int b, int lane, u64 epoch) {
    const char* fb = ws + WS_FLAG + off + (b & 7) * 2048;
    const unsigned foff = lane * 16, lo = (unsigned)epoch, hi = (unsigned)(epoch >> 32);
#pragma unroll 1
    for (unsigned s = 0; s < SPIN_CAP; ++s) {
        u32x4 f0, f1;
        asm volatile("global_load_dwordx4 %0, %2, %3 sc1\n\tglobal_load_dwordx4 %1, %2, %3 offset:1024 sc1\n\ts_waitcnt vmcnt(0)"
                     : "=&v"(f0), "=&v"(f1) : "v"(foff), "s"(fb) : "memory");
        const unsigned d = ((f0[0] ^ lo) | (f0[2] ^ lo) | (f1[0] ^ lo) | (f1[2] ^ lo)) | ((f0[1] ^ hi) | (f0[3] ^ hi) | (f1[1] ^ hi) | (f1[3] ^ hi));
        if (__all(d == 0u)) break;
        __builtin_amdgcn_s_sleep(2);
    }
    asm volatile("" ::: "memory");
}

DEVI unsigned out_off(int tile64, int d0, int w, int lane) {
    asm volatile("" : "+v"(lane));
    const int n = lane & 15, g = lane >> 4, T = w & 3, dp = w >> 2;
    const unsigned tok = 32u * (32 * (T >> 1) + 8 * (n >> 2) + 4 * (T & 1) + (n & 3)) + tile64;
    return tok * 1024u + (d0 + 32 * dp + 8 * g) * 2;
}
DEVI void h_epilogue(const f32x4& a0, const f32x4& a1, unsigned csa, float mb, float rr, __amdgpu_buffer_rsrc_t rh, unsigned off) {
    const f32x4 c0 = *(LAS f32x4*)(csa), c1 = *(LAS f32x4*)(csa + 64);
    float v[8];
#pragma unroll
    for (int r = 0; r < 4; ++r) {
        v[r] = fmaxf(rr * (a0[r] - mb * c0[r]), 0.0f);
        v[4 + r] = fmaxf(rr * (a1[r] - mb * c1[r]), 0.0f);
    }
    const u32x4 o = {pk2(v[0], v[1]), pk2(v[2], v[3]), pk2(v[4], v[5]), pk2(v[6], v[7])};
    st_sc1(o, rh, off);
}

template <int S> struct IC { static constexpr int v = S; };
constexpr int H_RING = 6;
DEVI unsigned y_blk(int i, unsigned yrot) { const unsigned t = yrot + i; return (t >= 14) ? t - 14 : t; }
constexpr int y_slot(int i) { return i < 4 ? i : i < 7 ? i + 1 : i < 12 ? i - 7 : i - 12; }
template <int I>
DEVI void w2_step(const char* wb, unsigned voffw, unsigned sd, unsigned yrot) {
    const char* sb = wb + (size_t)(y_blk(I, yrot) * (128u * 1024u));
    dmaq<y_slot(I) * 16384>(voffw, sb, sd);
    dmaq<y_slot(I) * 16384 + 1024>(voffw + 8192, sb, sd);
    dmaq<y_slot(I) * 16384 + 2048>(voffw + 16384, sb, sd);
    dmaq<y_slot(I) * 16384 + 3072>(voffw + 24576, sb, sd);
}
DEVI void f_gen(int fg, unsigned foff, int rot, int u, int lane) {
    const int n = lane & 15, g = lane >> 4;
    const int c = 16 * fg + n;
    const int k2 = (c == 1) ? 256 : (c >> 1), part = (c == 1) ? 0 : (c & 1);
    const float st = (float)k2 * (1.0f / 512.0f), ph = (float)part * 0.25f;
    const unsigned sg = (k2 & 1) ? 0x80008000u : 0u;
    const float d0 = (float)(64 * u + 8 * g + 64 * rot);
#pragma unroll
    for (int e = 0; e < 2; ++e) {
        const float x0 = __builtin_amdgcn_fractf(st * (d0 + 32.0f * e) + ph);
        float v[8];
#pragma unroll
        for (int j = 0; j < 8; ++j) v[j] = cosr(x0 + (float)j * st);
        const u32x4 o = {pk2(v[0], v[1]), pk2(v[2], v[3]), pk2(v[4], v[5]), pk2(v[6], v[7])};
        const u32x4 o2 = {o[0] ^ sg, o[1] ^ sg, o[2] ^ sg, o[3] ^ sg};
        lds_wr16(foff + (2 * u + e) * 1024 + lane * 16, o);
        lds_wr16(foff + (2 * u + e + 8) * 1024 + lane * 16, o2);
    }
}
template <int NF, int NG>
DEVI void m_first(const char* x, char* ws, u32x4 (&xr)[16][2], float (&mb)[2], float (&rr)[2], __amdgpu_buffer_rsrc_t rh, unsigned hoff,
                  int P, int w, int lane, int fg0, int rot) {
    const int n = lane & 15, g = lane >> 4, q = n >> 2, p = n & 3;
    const int t0 = 2 * P + (w >> 1);
    unsigned vx[4];
    {
        const unsigned r8 = lane >> 3, c8 = lane & 7;
        const unsigned v0 = (32u * (32 * (w & 1) + 8 * (r8 >> 2) + (r8 & 3)) + t0) * 1024u + ((c8 ^ (r8 >> 1)) * 16);
        vx[0] = v0; vx[1] = (v0 ^ 64u) + 524288u; vx[2] = v0 + 131072u; vx[3] = (v0 ^ 64u) + 524288u + 131072u;
    }
    const unsigned sdx = L_W1B + w * 4096;
    auto dmx = [&](auto J) {
        constexpr int j = decltype(J)::v;
        const char* sb = x + (((j + rot) & 7) << 7);
        dmaq<(j & 3) * 16384>(vx[0], sb, sdx); dmaq<(j & 3) * 16384 + 1024>(vx[1], sb, sdx);
        dmaq<(j & 3) * 16384 + 2048>(vx[2], sb, sdx); dmaq<(j & 3) * 16384 + 3072>(vx[3], sb, sdx);
    };
    const unsigned xa0 = sdx + n * 128 + ((g ^ ((n >> 1) & 7)) * 16), xa1 = xa0 ^ 64u;
    auto rdx = [&](int j) {
        xr[2 * j][0] = lds_rd16(xa0 + (j & 3) * 16384); xr[2 * j][1] = lds_rd16(xa0 + (j & 3) * 16384 + 2048);
        xr[2 * j + 1][0] = lds_rd16(xa1 + (j & 3) * 16384); xr[2 * j + 1][1] = lds_rd16(xa1 + (j & 3) * 16384 + 2048);
    };
    dmx(IC<0>{}); dmx(IC<1>{});
    if (NF == 3) { f_gen(fg0 + 1, L_F1, rot, w, lane); f_gen(fg0 + 2, L_F1 + 16384, rot, w, lane); }
    else f_gen(fg0, L_F0, rot, w, lane);
    dmx(IC<2>{}); dmx(IC<3>{});
    STAMP(1);
    WAIT_VM(12); rdx(0); WAIT_LGKM0();
    f32x4 acc[2][NG], af[2][NF], aS[2], aQ[2];
    u32x4 ones = {0x3f803f80u, 0x3f803f80u, 0x3f803f80u, 0x3f803f80u};
    PIN4(ones);
    {
        constexpr unsigned PITCH = (NG == 4) ? 128 : 64;
        const unsigned swz = (NG == 4) ? ((q >> 1) | ((g & 1) << 1)) : (g & 1);
        const unsigned abase = (8 * g + q) * PITCH + swz * 32 + p * 8;
        u32x4 A[NG], F[NF];
        auto rdA = [&](int ks, int db) -> u32x4 { const unsigned a = (abase ^ (db << 5)) + ks * 32 * PITCH; return trread2(a, a + 4 * PITCH); };
        auto rdF = [&](int ks, int f) -> u32x4 { return lds_rd16(((f == 0) ? L_F0 : L_F1 + (f - 1) * 16384) + ks * 1024 + lane * 16); };
        __builtin_amdgcn_s_setprio(2);
        barrier();
        STAMP(2);
#pragma unroll
        for (int db = 0; db < NG; ++db) A[db] = rdA(0, db);
#pragma unroll
        for (int f = 0; f < NF; ++f) F[f] = rdF(0, f);
#pragma unroll
        for (int ks = 0; ks < 16; ++ks) {
            STAMP2(ks);
            if (ks == 0) STAMP4(84, 85); if (ks == 2) STAMP4(86, 87); if (ks == 4) STAMP4(88, 89); if (ks == 6) STAMP4(90, 91);
            if (ks == 8) STAMP4(92, 93); if (ks == 10) STAMP4(94, 95); if (ks == 12) STAMP4(96, 97); if (ks == 14) STAMP4(98, 99);
            if (ks == 1 || ks == 3 || ks == 7) barrier();
            if (ks == 0) dmx(IC<4>{});
            if (ks == 2) { WAIT_LGKM(7); dmx(IC<5>{}); }
            if (ks == 4) { WAIT_LGKM(7); dmx(IC<6>{}); }
            if (ks == 6) { WAIT_LGKM(7); dmx(IC<7>{}); }
            if (ks == 1 || ks == 3 || ks == 5 || ks == 7) { WAIT_VM(12); rdx((ks + 1) / 2); }
            if (ks == 9) { WAIT_VM(8); rdx(5); }
            if (ks == 11) { WAIT_VM(4); rdx(6); }
            if (ks == 13) { WAIT_VM(0); rdx(7); }
            if ((ks == 7 || ks == 9 || ks == 11 || ks == 13) && lane == 0)
                (void)__hip_atomic_fetch_add((LAS unsigned*)SY_XR, 1u, __ATOMIC_RELAXED, __HIP_MEMORY_SCOPE_WORKGROUP);
#pragma unroll
            for (int db = 0; db < 4; ++db) {
                if (db < NG) {
                    mfma_ip(acc[0][db], A[db], xr[ks][0], ks == 0);
                    mfma_ip(acc[1][db], A[db], xr[ks][1], ks == 0);
                    if (ks < 15) A[db] = rdA(ks + 1, db);
                }
                if (db < 2) mfma_ip(aS[db], ones, xr[ks][db], ks == 0);
                else mfma_ip(aQ[db - 2], xr[ks][db - 2], xr[ks][db - 2], ks == 0);
                __builtin_amdgcn_sched_barrier(0);
            }
#pragma unroll
            for (int f = 0; f < NF; ++f) {
                mfma_ip(af[0][f], xr[ks][0], F[f], ks == 0);
                mfma_ip(af[1][f], xr[ks][1], F[f], ks == 0);
                if (ks < 15) F[f] = rdF(ks + 1, f);
                __builtin_amdgcn_sched_barrier(0);
            }
        }
    }
    MFMA_DONE();
#pragma unroll
    for (int tt = 0; tt < 2; ++tt) {
        PIN4(aS[tt]); PIN4(aQ[tt]);
#pragma unroll
        for (int i = 0; i < NG; ++i) PIN4(acc[tt][i]);
#pragma unroll
        for (int f = 0; f < NF; ++f) PIN4(af[tt][f]);
    }
    STAMP(3);
#pragma unroll
    for (int tt = 0; tt < 2; ++tt) {
        const float sq = (p & 2) ? ((p & 1) ? aQ[tt][3] : aQ[tt][2]) : ((p & 1) ? aQ[tt][1] : aQ[tt][0]);
        const float mean = aS[tt][0] * (1.0f / 512.0f);
        const float var = fmaxf(sq * (1.0f / 512.0f) - mean * mean, 0.0f);
        const float m = bf_round(mean);
        const float den = bf_round(__builtin_sqrtf(bf_round(bf_round(var) + 1e-5f)));
        const float r = 1.0f / den;
        if (g == q) lds_wr8(L_ST + (w * 32 + tt * 16 + n) * 8, u32x2{__builtin_bit_cast(unsigned, m), __builtin_bit_cast(unsigned, r)});
    }
    WAIT_LGKM0();
    {
        f32x4 st[2][2];
#pragma unroll
        for (int tt = 0; tt < 2; ++tt) {
            st[tt][0] = *(LAS f32x4*)(L_ST + (w * 32 + tt * 16 + 4 * g) * 8);
            st[tt][1] = *(LAS f32x4*)(L_ST + (w * 32 + tt * 16 + 4 * g) * 8 + 16);
            const f32x2 s = *(LAS f32x2*)(L_ST + (w * 32 + tt * 16 + n) * 8);
            mb[tt] = s[0]; rr[tt] = s[1];
        }
        const __amdgpu_buffer_rsrc_t rx = rsrc(ws + WS_X1T, 512 * 4096);
#pragma unroll
        for (int f = 0; f < NF; ++f) {
            const int c = 16 * (fg0 + f) + n;
            const float cs = (c == 0) ? 512.0f : 0.0f;
            float v[8];
#pragma unroll
            for (int tt = 0; tt < 2; ++tt)
#pragma unroll
                for (int r = 0; r < 4; ++r) v[4 * tt + r] = st[tt][r >> 1][2 * (r & 1) + 1] * (af[tt][f][r] - st[tt][r >> 1][2 * (r & 1)] * cs);
            const u32x4 o = {pk2(v[0], v[1]), pk2(v[2], v[3]), pk2(v[4], v[5]), pk2(v[6], v[7])};
            st_sc1(o, rx, (c >> 1) * 8192 + (4 * P + w) * 128 + (c & 1) * 64 + g * 16);
        }
    }
    STAMP(5);
    __builtin_amdgcn_s_setprio(0);
    barrier();
    STAMP(4);
#pragma unroll
    for (int i2 = 0; i2 < NG / 2; ++i2)
#pragma unroll
        for (int tt = 0; tt < 2; ++tt)
            h_epilogue(acc[tt][2 * i2], acc[tt][2 * i2 + 1], L_CS + (32 * i2 + 4 * g) * 4, mb[tt], rr[tt], rh, hoff + (i2 * 4 + tt) * 1024);
}

DEVI void m_phase_a(const char* x, char* ws, const char* wb, unsigned voffw, int b, int P, bool typeA, int w, int lane, int ii0, int fg0, int hj, int rot, u64& epoch) {
    const int n = lane & 15, g = lane >> 4, q = n >> 2, p = n & 3;
    const u64 ereq = epoch_request(ws);
    u32x4 xr[16][2];
    float mb[2], rr[2];
    const __amdgpu_buffer_rsrc_t rh = rsrc(ws + WS_H, 8u << 20);
    const unsigned hoff = (((2 * P + (w >> 1)) * 56 + ii0) * 4 + 2 * (w & 1)) * 1024 + lane * 16;
    if (typeA) m_first<1, 4>(x, ws, xr, mb, rr, rh, hoff, P, w, lane, fg0, rot);
    else m_first<3, 2>(x, ws, xr, mb, rr, rh, hoff, P, w, lane, fg0, rot);
    if (typeA) WAIT_VM(4); else WAIT_VM(2);
    epoch = epoch_finish(ereq);
    if (lds_arrive(SY_AARR, lane) == 7) post_replicas(ws, FL_A, b, lane, epoch);
    lds_wait_ge(SY_BOK, 4);
    STAMP(6); STAMPC(12);
    f32x4 a2[2][4];
    {
        const unsigned swz = (q >> 1) | ((g & 1) << 1);
        const unsigned abase = L_W1B + (8 * g + q) * 128 + swz * 32 + p * 8;
        u32x2 Al[8], Ah[8];
        auto adr = [&](int j) -> unsigned { return (abase ^ ((j & 3) << 5)) + (j >> 2) * 4096; };
#pragma unroll
        for (int j = 0; j < 7; ++j) { Al[j] = trread(adr(j)); Ah[j] = trread(adr(j) + 512); }
        __builtin_amdgcn_sched_barrier(0);
#pragma unroll
        for (int j = 0; j < 64; ++j) {
            if ((j & 3) == 0) { if (j + 7 < 64) WAIT_LGKM(6); else WAIT_LGKM(0); }
            const u32x4 a = {Al[j & 7][0], Al[j & 7][1], Ah[j & 7][0], Ah[j & 7][1]};
            mfma_ip(a2[0][j & 3], a, xr[j >> 2][0], j < 4);
            __builtin_amdgcn_sched_barrier(0);
            if (j + 7 < 64) Al[(j + 7) & 7] = trread(adr(j + 7));
            __builtin_amdgcn_sched_barrier(0);
            mfma_ip(a2[1][j & 3], a, xr[j >> 2][1], j < 4);
            __builtin_amdgcn_sched_barrier(0);
            if (j + 7 < 64) Ah[(j + 7) & 7] = trread(adr(j + 7) + 512);
            __builtin_amdgcn_sched_barrier(0);
        }
        MFMA_DONE();
#pragma unroll
        for (int db = 0; db < 4; ++db) { PIN4(a2[0][db]); PIN4(a2[1][db]); }
    }
    WAIT_LGKM0();
    (void)lds_arrive(SY_H47, lane);
    STAMP(7); STAMPC(13);
    lds_wait_ge(SY_CS2, 4);
    const unsigned hoff2 = hoff + (typeA ? 8192 : 4096);
#pragma unroll
    for (int i2 = 0; i2 < 2; ++i2)
#pragma unroll
        for (int tt = 0; tt < 2; ++tt)
            h_epilogue(a2[tt][2 * i2], a2[tt][2 * i2 + 1], L_CS + (64 + 32 * i2 + 4 * g) * 4, mb[tt], rr[tt], rh, hoff2 + (i2 * 4 + tt) * 1024);
    WAIT_VM(0);
    if (lds_arrive(SY_HARR, lane) == 3 && lane == 0)
        __hip_atomic_store((GAS u64*)(ws + WS_FLAG + FL_H + P * 128 + hj * 8), epoch, __ATOMIC_RELAXED, __HIP_MEMORY_SCOPE_AGENT);
    STAMP(8);
}

#define GLOAD16(dst, voff, sbase, imm) asm volatile("global_load_dwordx4 %0, %1, %2 offset:%3 sc1" : "=v"(dst) : "v"(voff), "s"(sbase), "n"(imm) : "memory")
constexpr int y_ops(int j) { return ((j == 4 || j == 5) ? 4 : 0) + ((j + H_RING < 14) ? 4 : 0); }
constexpr int y_after(int i) {
    int cnt = (i < H_RING) ? 4 * (H_RING - 1 - i) : 0;
    for (int j = (i < H_RING) ? 0 : (i - (H_RING - 1)); j < i; ++j) cnt += y_ops(j);
    if (i == 8 || i == 9) {
        int c2 = (i - 4 + H_RING < 14) ? 4 : 0;
        for (int j = i - 3; j < i; ++j) c2 += y_ops(j);
        if (c2 < cnt) cnt = c2;
    }
    return cnt;
}

DEVI void m_team(const char* x, const char* __restrict__ w2, char* ws, int b, int P, bool typeA, int ii0, int fg0, int hj,
                 int tile64, int d0, int rot, unsigned yrot, int w, int lane, u32x4& xv) {
    const int n = lane & 15, g = lane >> 4, q = n >> 2, p = n & 3;
    const int r8 = lane >> 3, p16 = lane & 7;
    const unsigned voffw = r8 * 1024 + ((p16 ^ (2 * ((r8 >> 1) & 3))) * 16);
    const char* wb = w2 + d0 * 2 + (size_t)w * 32 * 1024;
    u64 epoch;
    m_phase_a(x, ws, wb, voffw, b, P, typeA, w, lane, ii0, fg0, hj, rot, epoch);

    const unsigned sdw = w * 4096;
    const unsigned abase = w * 4096 + (4 * g + q) * 128 + (((2 * g + (q >> 1)) & 3) * 32) + p * 8;
    u32x4 A[4][4];
    auto rdA = [&](int i) {
#pragma unroll
        for (int db = 0; db < 4; ++db) { const unsigned a = (abase ^ (db << 5)) + y_slot(i) * 16384; A[i & 3][db] = trread2(a, a + 2048); }
    };
    lds_wait_ge(SY_W2OK, 4);
    rdA(0); rdA(1); rdA(2); rdA(3);
    lds_wait_ge(SY_H47, 4);
    w2_step<4>(wb, voffw, sdw, yrot); w2_step<5>(wb, voffw, sdw, yrot); w2_step<6>(wb, voffw, sdw, yrot); w2_step<11>(wb, voffw, sdw, yrot);
#pragma unroll
    for (int i = 0; i < 4; ++i)
#pragma unroll
        for (int db = 0; db < 4; ++db) PIN4(A[i][db]);
    WAIT_LGKM0();
    w2_step<7>(wb, voffw, sdw, yrot); w2_step<8>(wb, voffw, sdw, yrot); w2_step<9>(wb, voffw, sdw, yrot); w2_step<10>(wb, voffw, sdw, yrot);
    {
        const char* fb = ws + WS_FLAG + FL_H + (tile64 >> 1) * 128;
        const unsigned foff = (lane & 15) * 8, lo = (unsigned)epoch, hi = (unsigned)(epoch >> 32);
#pragma unroll 1
        for (unsigned s = 0; s < SPIN_CAP; ++s) {
            u32x2 f;
            asm volatile("global_load_dwordx2 %0, %1, %2 sc1\n\ts_waitcnt vmcnt(0)" : "=v"(f) : "v"(foff), "s"(fb) : "memory");
            if (__all(((f[0] ^ lo) | (f[1] ^ hi)) == 0u)) break;
            __builtin_amdgcn_s_sleep(2);
        }
        asm volatile("" ::: "memory");
    }
    STAMP(9); STAMP3(15);
    const char* hb = ws + WS_H + (size_t)(tile64 * 56 + w) * 4096;
    const unsigned voffh = lane * 16;
    u32x4 h[H_RING][4];
    auto issue_h = [&](int i) {
        const char* hp = hb + (size_t)(y_blk(i, yrot) * 16384u);
        GLOAD16(h[i % H_RING][0], voffh, hp, 0); GLOAD16(h[i % H_RING][1], voffh, hp, 1024);
        GLOAD16(h[i % H_RING][2], voffh, hp, 2048); GLOAD16(h[i % H_RING][3], voffh, hp, 3072);
    };
#pragma unroll
    for (int i = 0; i < H_RING; ++i) issue_h(i);
    f32x4 ay[4][4];
#define T1_WAIT(N, slot) asm volatile("s_waitcnt vmcnt(%4)" : "+v"(h[slot][0]), "+v"(h[slot][1]), "+v"(h[slot][2]), "+v"(h[slot][3]) : "n"(N) : "memory")
    auto ystep = [&](auto I) {
        constexpr int i = decltype(I)::v;
        T1_WAIT(y_after(i), i % H_RING);
        if (i == 0) STAMP(10);
        STAMP3(i);
#pragma unroll
        for (int db = 0; db < 4; ++db)
#pragma unroll
            for (int t = 0; t < 4; ++t) mfma_ip(ay[db][t], A[i & 3][db], h[i % H_RING][t], i == 0);
        __builtin_amdgcn_sched_barrier(0);
        if (i + 4 < 14) rdA(i + 4);
        if (i == 2) (void)lds_arrive(SY_Y6, lane);
        if constexpr (i == 4 || i == 5) { WAIT_LGKM0(); w2_step<i + 8>(wb, voffw, sdw, yrot); }
        if (i + H_RING < 14) issue_h(i + H_RING);
        if (i == 10) xv = *(const GAS u32x4*)((const GAS char*)x + out_off(tile64, d0, w, lane));
        __builtin_amdgcn_sched_barrier(0);
    };
    ystep(IC<0>{}); ystep(IC<1>{}); ystep(IC<2>{}); ystep(IC<3>{}); ystep(IC<4>{}); ystep(IC<5>{}); ystep(IC<6>{});
    ystep(IC<7>{}); ystep(IC<8>{}); ystep(IC<9>{}); ystep(IC<10>{}); ystep(IC<11>{}); ystep(IC<12>{}); ystep(IC<13>{});
    MFMA_DONE();
#pragma unroll
    for (int t = 0; t < 4; ++t)
#pragma unroll
        for (int db = 0; db < 4; ++db) PIN4(ay[db][t]);
    STAMP(11); STAMP3(14);
#pragma unroll
    for (int t = 0; t < 4; ++t)
#pragma unroll
        for (int db = 0; db < 4; ++db) *(LAS f32x4*)((t * 4 + w) * 4096 + db * 1024 + lane * 16) = ay[db][t];
}

template <bool HAS_I, bool FIRST>
DEVI void p2_unit(char* ws, const u32x4 (&Ur)[2][2], const u32x4 (&Ui)[2][2], const u32x4 (&Wf)[2], int zrow, int v, int lane, unsigned& sync_n,
                  const u32x4 (&C64)[2], const u32x4 (&S64)[2], const u32x4 (&N64)[2],
                  const u32x4 (&C32)[2], const u32x4 (&S32)[2], const u32x4 (&N32)[2],
                  const float (&tc)[8], const float (&tsn)[8]) {
    const int n = lane & 15, g = lane >> 4, q = n >> 2, p = n & 3;
    STAMP3(2);
    float br[8], bi[8];
#pragma unroll
    for (int mt = 0; mt < 2; ++mt) {
        f32x4 dr = {0, 0, 0, 0}, di = {0, 0, 0, 0};
#pragma unroll
        for (int ks = 0; ks < 2; ++ks) {
            dr = mfma(Ur[mt][ks], C64[ks], dr);
            di = mfma(Ur[mt][ks], N64[ks], di);
            if (HAS_I) { dr = mfma(Ui[mt][ks], S64[ks], dr); di = mfma(Ui[mt][ks], C64[ks], di); }
        }
#pragma unroll
        for (int r = 0; r < 4; ++r) {
            const float c = tc[4 * mt + r], s = tsn[4 * mt + r];
            br[4 * mt + r] = dr[r] * c + di[r] * s;
            bi[4 * mt + r] = di[r] * c - dr[r] * s;
        }
    }
    u32x4 Br = {pk2(br[0], br[1]), pk2(br[2], br[3]), pk2(br[4], br[5]), pk2(br[6], br[7])};
    u32x4 Bi = {pk2(bi[0], bi[1]), pk2(bi[2], bi[3]), pk2(bi[4], bi[5]), pk2(bi[6], bi[7])};
    float yr[8], yi[8];
#pragma unroll
    for (int t = 0; t < 2; ++t) {
        f32x4 dr = {0, 0, 0, 0}, di = {0, 0, 0, 0};
        dr = mfma(C32[t], Br, dr); dr = mfma(S32[t], Bi, dr);
        di = mfma(C32[t], Bi, di); di = mfma(N32[t], Br, di);
#pragma unroll
        for (int r = 0; r < 4; ++r) {
            const float wre = bf_lo(Wf[t][r]), wim = bf_hi(Wf[t][r]);
            yr[4 * t + r] = dr[r] * wre - di[r] * wim;
            yi[4 * t + r] = dr[r] * wim + di[r] * wre;
        }
    }
    u32x4 Yr = {pk2(yr[0], yr[1]), pk2(yr[2], yr[3]), pk2(yr[4], yr[5]), pk2(yr[6], yr[7])};
    u32x4 Yi = {pk2(yi[0], yi[1]), pk2(yi[2], yi[3]), pk2(yi[4], yi[5]), pk2(yi[6], yi[7])};
#pragma unroll
    for (int mt = 0; mt < 2; ++mt) {
        f32x4 dr = {0, 0, 0, 0}, di = {0, 0, 0, 0};
        dr = mfma(C32[mt], Yr, dr); dr = mfma(N32[mt], Yi, dr);
        di = mfma(C32[mt], Yi, di); di = mfma(S32[mt], Yr, di);
        float pr[4], pi[4];
#pragma unroll
        for (int r = 0; r < 4; ++r) {
            const float c = tc[4 * mt + r] * (1.0f / 2048.0f), s = tsn[4 * mt + r] * (1.0f / 2048.0f);
            pr[r] = dr[r] * c - di[r] * s;
            pi[r] = di[r] * c + dr[r] * s;
        }
        const unsigned a = L_BP + (16 * v + n) * 72 + (16 * mt + 4 * g) * 2;
        lds_wr8(a, u32x2{pk2(pr[0], pr[1]), pk2(pr[2], pr[3])});
        lds_wr8(a + 64 * 72, u32x2{pk2(pi[0], pi[1]), pk2(pi[2], pi[3])});
    }
    STAMP3(3);
    sync_n += 4; team_sync(SY_TEAM, sync_n, lane);
    STAMP3(4);
#pragma unroll
    for (int nt = 0; nt < 2; ++nt) {
        f32x4 dr = {0, 0, 0, 0}, di = {0, 0, 0, 0};
#pragma unroll
        for (int ks = 0; ks < 2; ++ks) {
            const unsigned a = L_BP + (32 * ks + 8 * g + q) * 72 + nt * 32 + p * 8;
            const u32x4 pr = trread2(a, a + 4 * 72), pi = trread2(a + 64 * 72, a + 64 * 72 + 4 * 72);
            dr = mfma(C64[ks], pr, dr); dr = mfma(N64[ks], pi, dr);
            if (HAS_I) { di = mfma(C64[ks], pi, di); di = mfma(S64[ks], pr, di); }
        }
        const unsigned a = L_ZS + (16 * nt + n) * 144 + (16 * v + 4 * g) * 2;
        lds_wr8(a, u32x2{pk2(dr[0], dr[1]), pk2(dr[2], dr[3])});
        if (HAS_I) lds_wr8(a + 32 * 144, u32x2{pk2(di[0], di[1]), pk2(di[2], di[3])});
    }
    STAMP3(5);
    sync_n += 4; team_sync(SY_TEAM, sync_n, lane);
    STAMP3(6);
    {
        const __amdgpu_buffer_rsrc_t rs = rsrc(ws + WS_ZT, 512 * 4096);
#pragma unroll
        for (int it = 0; it < (HAS_I ? 2 : 1); ++it) {
            const int row = 8 * v + (lane >> 3), ch = lane & 7;
            const u32x4 o = lds_rd16(L_ZS + (it * 32 + row) * 144 + ch * 16);
            st_sc1(o, rs, (zrow + it) * 4096 + row * 128 + ch * 16);
        }
    }
}

#define GLOAD_U16(dst, voff, sbase, imm) asm volatile("global_load_ushort %0, %1, %2 offset:%3" : "=v"(dst) : "v"(voff), "s"(sbase), "n"(imm) : "memory")
DEVI void g_gen(u32x4 (&G)[4], int dbase, int v, int lane) {
    const int n = lane & 15, g = lane >> 4;
    const int d = dbase + n;
    float c[4], s[4];
#pragma unroll
    for (int jj = 0; jj < 4; ++jj) {
        const float a = (float)(((64 * v + 4 * g + jj) * d) & 511) * (1.0f / 512.0f);
        c[jj] = cosr(a); s[jj] = sinr(a);
    }
    const float ar = (float)((16 * d) & 511) * (1.0f / 512.0f);
    const float cr = cosr(ar), sr = sinr(ar);
#pragma unroll
    for (int ks = 0; ks < 4; ++ks) {
        u32x4 o;
#pragma unroll
        for (int jj = 0; jj < 4; ++jj) o[jj] = pk2(c[jj], -s[jj]);
        if (ks == 0 && v == 0 && g == 0) o[0] = pk2(0.5f, (d & 1) ? -0.5f : 0.5f);
        G[ks] = o;
        PIN4(G[ks]);
        if (ks < 3) {
#pragma unroll
            for (int jj = 0; jj < 4; ++jj) {
                const float c2 = c[jj] * cr - s[jj] * sr, s2 = s[jj] * cr + c[jj] * sr;
                c[jj] = c2; s[jj] = s2;
            }
        }
    }
}
DEVI void s_team(const char* x, const char* __restrict__ w1, const char* __restrict__ w2, const unsigned short* wr, const unsigned short* wi, char* ws, int b,
                 bool typeA, int col0, int fg0, int tile64, int d0, int rot, unsigned yrot, int u, int lane, u32x4& xv) {
    const int n = lane & 15, g = lane >> 4, q = n >> 2, p = n & 3;
    const u64 ereq = epoch_request(ws);
    const char* wb = w1 + col0 * 2;
    const int r8 = lane >> 3, p16 = lane & 7;
    const unsigned v128 = r8 * 3584 + (p16 & 1) * 16;
    unsigned va[2];
    if (typeA) {
#pragma unroll
        for (int j = 0; j < 2; ++j) va[j] = v128 + (((p16 >> 1) ^ (((r8 >> 1) & 1) | (j << 1))) * 32) + (2 * u + j) * 8 * 3584;
    } else {
        const int r16 = lane >> 2, c4 = lane & 3;
        va[0] = va[1] = r16 * 3584 + (((c4 >> 1) ^ (r16 >> 3)) * 32) + (c4 & 1) * 16 + u * 16 * 3584;
    }
    const unsigned sdA = typeA ? u * 2048 : u * 1024;
    auto stage_a = [&](auto S) {
        constexpr int s = decltype(S)::v;
        const char* sb = wb + (size_t)(((s + rot) & 7) * (64 * 3584));
        if (typeA) { dmaq<s * 8192>(va[0], sb, sdA); dmaq<s * 8192 + 1024>(va[1], sb, sdA); }
        else dmaq<s * 4096>(va[0], sb, sdA);
    };
    const int rs = b & 63, cq = b >> 6;
    const int jv = rs & 3, ja = (rs >> 2) & 1, jt = (rs >> 3) & 1, jg = rs >> 4;
    unsigned wv[2][4][2], wsp;
    auto filter_loads = [&]() {
        const unsigned k0 = 16 * jv + 8 * ja + 2 * u + 64 * (16 * jt + 4 * jg);
        const unsigned off0 = (k0 * 257 + 64 * cq + lane) * 2;
#pragma unroll
        for (int r = 0; r < 4; ++r) {
            const unsigned off = off0 + r * (64 * 514);
            GLOAD_U16(wv[0][r][0], off, wr, 0); GLOAD_U16(wv[0][r][1], off, wi, 0);
            GLOAD_U16(wv[1][r][0], off, wr, 514); GLOAD_U16(wv[1][r][1], off, wi, 514);
        }
        const int unit = 2 * b + ((lane >> 3) & 1), reg = (lane >> 1) & 3;
        const unsigned k1 = 16 * (unit >> 7) + (unit & 15) + 64 * (16 * ((unit >> 6) & 1) + 4 * ((unit >> 4) & 3) + reg);
        const unsigned long long a = (unsigned long long)((lane & 1) ? wi : wr) + (k1 * 257 + 256) * 2;
        asm volatile("global_load_ushort %0, %1, off" : "=v"(wsp) : "v"(a) : "memory");
    };
    const char* wb2 = wb + (typeA ? 128 : 64);
    const unsigned vbb = v128 + (((p16 >> 1) ^ (((r8 >> 1) & 1) | ((u & 1) << 1))) * 32) + u * 8 * 3584;
    const unsigned sdB = L_W1B + u * 1024;
    auto stage_b = [&](auto K) {
        constexpr int k = decltype(K)::v;
        dmaq<k * 4096>(vbb, wb2 + (size_t)(((k + 2 * rot) & 15) * (32 * 3584)), sdB);
    };
    u32x4 C64[2], S64[2], N64[2], C32[2], S32[2], N32[2];
    float tc[8], tsn[8];
    auto gen_c64 = [&]() {
        const int m = 16 * u + n;
        const float st = (float)m * (1.0f / 64.0f), a0 = __builtin_amdgcn_fractf(st * (float)(8 * g));
        float c[8], sn[8];
#pragma unroll
        for (int j = 0; j < 8; ++j) { const float a = a0 + (float)j * st; c[j] = cosr(a); sn[j] = sinr(a); }
        const unsigned sg = (m & 1) ? 0x80008000u : 0u;
#pragma unroll
        for (int e = 0; e < 4; ++e) {
            C64[0][e] = pk2(c[2 * e], c[2 * e + 1]); S64[0][e] = pk2(sn[2 * e], sn[2 * e + 1]); N64[0][e] = S64[0][e] ^ 0x80008000u;
            C64[1][e] = C64[0][e] ^ sg; S64[1][e] = S64[0][e] ^ sg; N64[1][e] = N64[0][e] ^ sg;
        }
#pragma unroll
        for (int e = 0; e < 2; ++e) { PIN4(C64[e]); PIN4(S64[e]); PIN4(N64[e]); }
    };
    auto gen_c32 = [&]() {
        const float st = (float)n * (1.0f / 32.0f), a0 = st * (float)(4 * g);
        float c[4], sn[4];
#pragma unroll
        for (int j = 0; j < 4; ++j) { const float a = a0 + (float)j * st; c[j] = cosr(a); sn[j] = sinr(a); }
        const unsigned sg = (n & 1) ? 0x80008000u : 0u;
#pragma unroll
        for (int e = 0; e < 2; ++e) {
            C32[0][e] = pk2(c[2 * e], c[2 * e + 1]); S32[0][e] = pk2(sn[2 * e], sn[2 * e + 1]);
            C32[0][e + 2] = C32[0][e] ^ sg; S32[0][e + 2] = S32[0][e] ^ sg;
        }
#pragma unroll
        for (int e = 0; e < 4; ++e) {
            C32[1][e] = C32[0][e] ^ 0x80000000u; S32[1][e] = S32[0][e] ^ 0x80000000u;
            N32[0][e] = S32[0][e] ^ 0x80008000u; N32[1][e] = S32[0][e] ^ 0x00008000u;
        }
#pragma unroll
        for (int e = 0; e < 2; ++e) { PIN4(C32[e]); PIN4(S32[e]); PIN4(N32[e]); }
    };
    auto gen_tw = [&]() {
        const float st = (float)(16 * u + n) * (1.0f / 2048.0f), a0 = st * (float)(4 * g);
#pragma unroll
        for (int j = 0; j < 8; ++j) { const float a = a0 + (float)(16 * (j >> 2) + (j & 3)) * st; tc[j] = cosr(a); tsn[j] = sinr(a); }
#pragma unroll
        for (int j = 0; j < 8; ++j) { PIN4(tc[j]); PIN4(tsn[j]); }
    };
    const u32x4 ones = {0x3f803f80u, 0x3f803f80u, 0x3f803f80u, 0x3f803f80u};
    {
        const unsigned swz = (q >> 1) | ((g & 1) << 1);
        const unsigned abase = typeA ? (((8 * g + q) * 128 + swz * 32 + p * 8) ^ (u << 5)) : ((8 * g + q) * 64 + (((u ^ g) & 1) * 32) + p * 8);
        const unsigned kpitch = typeA ? 4096 : 2048, hpitch = typeA ? 512 : 256;
        f32x4 cs = {0, 0, 0, 0};
        auto col_sums = [&](int s) {
            const unsigned a0 = abase + (2 * s) * kpitch, a1 = a0 + kpitch;
            const u32x4 c0 = trread2(a0, a0 + hpitch), c1 = trread2(a1, a1 + hpitch);
            cs = mfma(c0, ones, cs);
            cs = mfma(c1, ones, cs);
        };
        stage_a(IC<0>{}); stage_a(IC<1>{});
        if (!typeA) f_gen(fg0, L_F0, rot, u, lane);
        stage_a(IC<2>{}); stage_a(IC<3>{});
        STAMP(1);
        if (typeA) WAIT_VM(6); else WAIT_VM(3);
        WAIT_LGKM0();
        STAMP(2);
        barrier();
        STAMP(3);
        filter_loads();
        stage_a(IC<4>{}); stage_a(IC<5>{});
        col_sums(0);
        if (typeA) WAIT_VM(25); else WAIT_VM(21);
        barrier();
        stage_a(IC<6>{}); stage_a(IC<7>{});
        col_sums(1);
        if (typeA) WAIT_VM(25); else WAIT_VM(21);
        barrier();
        col_sums(2); col_sums(3);
        WAIT_VM(0);
        asm volatile("" : "+v"(wv[0][0][0]), "+v"(wv[0][0][1]), "+v"(wv[0][1][0]), "+v"(wv[0][1][1]), "+v"(wv[0][2][0]), "+v"(wv[0][2][1]), "+v"(wv[0][3][0]), "+v"(wv[0][3][1]),
                          "+v"(wv[1][0][0]), "+v"(wv[1][0][1]), "+v"(wv[1][1][0]), "+v"(wv[1][1][1]), "+v"(wv[1][2][0]), "+v"(wv[1][2][1]), "+v"(wv[1][3][0]), "+v"(wv[1][3][1]), "+v"(wsp));
#pragma unroll
        for (int e = 0; e < 2; ++e) {
            u32x4 o;
#pragma unroll
            for (int r = 0; r < 4; ++r) o[r] = wv[e][r][0] | (wv[e][r][1] << 16);
            lds_wr16(L_WS + lane * 144 + (2 * u + e) * 16, o);
        }
        WAIT_LGKM0();
        barrier();
        {
            const __amdgpu_buffer_rsrc_t rw = rsrc(ws + WS_WT, 257 * 8192);
#pragma unroll
            for (int e = 0; e < 2; ++e) {
                const int k2l = 16 * u + 8 * e + (lane >> 3);
                const u32x4 o = lds_rd16(L_WS + k2l * 144 + (lane & 7) * 16);
                st_sc1(o, rw, (64 * cq + k2l) * 8192 + ((jv * 2 + jt) * 64 + 16 * jg + 8 * ja + (lane & 7)) * 16);
            }
            if (u == 0 && lane < 16) __builtin_amdgcn_raw_buffer_store_b16((unsigned short)wsp, rw, 256 * 8192 + 2 * b * 16 + lane * 2, 0, 16);
        }
        col_sums(4); col_sums(5); col_sums(6); col_sums(7);
        if (n == 0 && (typeA || u < 2)) *(LAS f32x4*)(L_CS + (16 * u + 4 * g) * 4) = cs;
        lds_wait_ge(SY_XR, 4);  stage_b(IC<0>{});  stage_b(IC<1>{});  stage_b(IC<2>{});  stage_b(IC<3>{});
        lds_wait_ge(SY_XR, 8);  stage_b(IC<4>{});  stage_b(IC<5>{});  stage_b(IC<6>{});  stage_b(IC<7>{});
        lds_wait_ge(SY_XR, 12); stage_b(IC<8>{});  stage_b(IC<9>{});  stage_b(IC<10>{}); stage_b(IC<11>{});
        lds_wait_ge(SY_XR, 16); stage_b(IC<12>{}); stage_b(IC<13>{}); stage_b(IC<14>{}); stage_b(IC<15>{});
    }
    WAIT_LGKM0();
    barrier();
    STAMP(4);
    WAIT_VM(0);
    (void)lds_arrive(SY_BOK, lane);
    const u64 epoch = epoch_finish(ereq);
    if (lds_arrive(SY_AARR, lane) == 7) post_replicas(ws, FL_A, b, lane, epoch);
    lds_wait_ge(SY_BOK, 4);
    STAMP(5);
    {
        f32x4 cs = {0, 0, 0, 0};
        const unsigned swz = (q >> 1) | ((g & 1) << 1);
        const unsigned abase = L_W1B + (((8 * g + q) * 128 + swz * 32 + p * 8) ^ (u << 5));
#pragma unroll
        for (int ks = 0; ks < 16; ++ks) { const unsigned a = abase + ks * 4096; cs = mfma(trread2(a, a + 512), ones, cs); }
        if (n == 0) *(LAS f32x4*)(L_CS + (64 + 16 * u + 4 * g) * 4) = cs;
    }
    WAIT_LGKM0();
    (void)lds_arrive(SY_CS2, lane);
    {
        const unsigned voffw = r8 * 1024 + ((p16 ^ (2 * ((r8 >> 1) & 3))) * 16);
        const char* wb3 = w2 + d0 * 2 + (size_t)u * 32 * 1024;
        const unsigned sd = u * 4096;
        w2_step<0>(wb3, voffw, sd, yrot); w2_step<1>(wb3, voffw, sd, yrot); w2_step<2>(wb3, voffw, sd, yrot); w2_step<3>(wb3, voffw, sd, yrot);
    }
    gen_tw(); gen_c64(); gen_c32();
    WAIT_VM(0);
    (void)lds_arrive(SY_W2OK, lane);
    STAMP(6);
    __builtin_amdgcn_s_setprio(1);

    const int v = u;
    if (v == 0) {
        poll256(ws, FL_A, b, lane, epoch);
        if (lane == 0) __hip_atomic_store((LAS unsigned*)SY_AOK, 1u, __ATOMIC_RELAXED, __HIP_MEMORY_SCOPE_WORKGROUP);
    } else lds_wait_ge(SY_AOK, 1u);
    STAMP(7); STAMP3(1);
    u32x4 Ur[2][2], Ui[2][2], Wf[2], Wg[2];
    unsigned sync_n = 0;
    {
        const char* xb = ws + WS_X1T + (size_t)(b * 8192 + v * 2048);
        const char* fb = ws + WS_WT + (size_t)(b * 8192 + v * 2048);
        const char* fb2 = ws + WS_WT + (size_t)(256 * 8192 + v * 2048);
        const unsigned vo = lane * 16;
        u32x4 u0, u1;
        GLOAD16(u0, vo, xb, 0); GLOAD16(u1, vo, xb, 1024);
        GLOAD16(Wf[0], vo, fb, 0); GLOAD16(Wf[1], vo, fb, 1024);
        if (b == 0) { GLOAD16(Wg[0], vo, fb2, 0); GLOAD16(Wg[1], vo, fb2, 1024); }
        else { Wg[0] = u32x4{0, 0, 0, 0}; Wg[1] = u32x4{0, 0, 0, 0}; }
        asm volatile("s_waitcnt vmcnt(0)" : "+v"(u0), "+v"(u1), "+v"(Wf[0]), "+v"(Wf[1]), "+v"(Wg[0]), "+v"(Wg[1]) :: "memory");
        const unsigned ta = 8 * v + g, tb = ta + 4;
        lds_wr16(L_ZS + ta * 256 + ((n ^ ta) & 15) * 16, u0);
        lds_wr16(L_ZS + tb * 256 + ((n ^ tb) & 15) * 16, u1);
        sync_n += 4; team_sync(SY_TEAM, sync_n, lane);
#pragma unroll
        for (int mt = 0; mt < 2; ++mt)
#pragma unroll
            for (int ks = 0; ks < 2; ++ks) {
                const unsigned a = L_ZS + (16 * mt + n) * 256;
                Ur[mt][ks] = lds_rd16(a + ((8 * ks + g) ^ n) * 16);
                Ui[mt][ks] = lds_rd16(a + ((8 * ks + 4 + g) ^ n) * 16);
            }
    }
    if (b != 0) p2_unit<true, true>(ws, Ur, Ui, Wf, 2 * b, v, lane, sync_n, C64, S64, N64, C32, S32, N32, tc, tsn);
    else {
        p2_unit<false, true>(ws, Ur, Ur, Wf, 0, v, lane, sync_n, C64, S64, N64, C32, S32, N32, tc, tsn);
        p2_unit<false, false>(ws, Ui, Ui, Wg, 1, v, lane, sync_n, C64, S64, N64, C32, S32, N32, tc, tsn);
    }
    STAMP(8); STAMP3(7);
    WAIT_VM(0);
    if (lds_arrive(SY_ZARR, lane) == 3) post_replicas(ws, FL_Z, b, lane, epoch);
    STAMP(9); STAMP3(9);
    xv = *(const GAS u32x4*)((const GAS char*)x + out_off(tile64, d0, u + 4, lane));
    u32x4 G[4][4];
    g_gen(G[0], d0, u, lane); g_gen(G[1], d0 + 16, u, lane); g_gen(G[2], d0 + 32, u, lane); g_gen(G[3], d0 + 48, u, lane);
    STAMP(10); STAMP3(10);
    if (v == 0) {
        poll256(ws, FL_Z, b, lane, epoch);
        if (lane == 0) {
            __hip_atomic_store((LAS unsigned*)SY_ZOK, 1u, __ATOMIC_RELAXED, __HIP_MEMORY_SCOPE_WORKGROUP);
            if (b == 0) __hip_atomic_store((GAS u64*)(ws + WS_FLAG), epoch, __ATOMIC_RELAXED, __HIP_MEMORY_SCOPE_AGENT);
        }
    } else lds_wait_ge(SY_ZOK, 1u);
    lds_wait_ge(SY_Y6, 4);
    STAMP(11); STAMP3(11);
    const unsigned zreg = L_Z + v * 16384;
    {
        const __amdgpu_buffer_rsrc_t rz = rsrc(ws + WS_ZT, 512 * 4096);
        const int r8 = lane >> 3, ch = lane & 7;
        u32x4 z[16];
#pragma unroll
        for (int i = 0; i < 16; ++i) z[i] = ld_sc1(rz, (128 * v + 8 * i + r8) * 4096 + tile64 * 128 + ch * 16);
#pragma unroll
        for (int i = 0; i < 16; ++i) lds_wr16(zreg + (8 * i + r8) * 128 + ((ch ^ (4 * ((r8 >> 1) & 1))) * 16), z[i]);
    }
    STAMP(12); STAMP3(12);
    f32x4 acc[4][4];
    {
        auto rdZ = [&](int j) -> u32x4 {
            const int ks = j >> 2, t = j & 3;
            const unsigned a = zreg + (32 * ks + 8 * g + q) * 128 + (((t >> 1) ^ ((q >> 1) & 1)) * 64) + p * 16 + (t & 1) * 8;
            return trread2(a, a + 512);
        };
        u32x4 z[4];
#pragma unroll
        for (int j = 0; j < 4; ++j) z[j] = rdZ(j);
        __builtin_amdgcn_sched_barrier(0);
#pragma unroll
        for (int j = 0; j < 16; ++j) {
#pragma unroll
            for (int db = 0; db < 4; ++db) mfma_ip(acc[db][j & 3], G[db][j >> 2], z[j & 3], j < 4);
            if (j + 4 < 16) z[j & 3] = rdZ(j + 4);
            __builtin_amdgcn_sched_barrier(0);
        }
    }
    MFMA_DONE();
#pragma unroll
    for (int t = 0; t < 4; ++t)
#pragma unroll
        for (int db = 0; db < 4; ++db) PIN4(acc[db][t]);
#pragma unroll
    for (int t = 0; t < 4; ++t)
#pragma unroll
        for (int db = 0; db < 4; ++db) *(LAS f32x4*)(zreg + (t * 4 + db) * 1024 + lane * 16) = acc[db][t];
    __builtin_amdgcn_s_setprio(0);
    STAMP(13); STAMP3(13);
}

#if defined(__HIP_DEVICE_COMPILE__)
#define NOPK __attribute__((target("no-packed-fp32-ops")))
#else
#define NOPK
#endif
__global__ NOPK void __launch_bounds__(512, 2) fused_kernel(const char* x, const unsigned short* wr, const unsigned short* wi,
                                                        const char* __restrict__ w1, const char* __restrict__ w2, char* ws,
                                                        char* __restrict__ out) {
    const int b = blockIdx.x, lane = threadIdx.x & 63, w = __builtin_amdgcn_readfirstlane(threadIdx.x >> 6);
    STAMP(0);
    if (w >= 4) STAMP3(0);
    const int xcd = b & 7, slot = b >> 3;
    if (w == 7 || w == 3) {
        unsigned long long pc;
        asm volatile("s_getpc_b64 %0" : "=s"(pc));
        const unsigned piece = (unsigned)(2 * slot + (w >> 2)) % CODE_PIECES;
        const unsigned voff = piece * 4096 + lane * 64;
        asm volatile("s_mov_b32 m0, %2\n\ts_nop 0\n\tglobal_load_lds_dword %0, %1" :: "v"(voff), "s"(pc), "s"(L_CS) : "memory");
    }
    const int hf = xcd & 1, P = 4 * (xcd >> 1) + (slot >> 3), i = slot & 7;
    const bool typeA = i < 4;
    const int col0 = 896 * hf + (typeA ? 128 * i : 512 + 96 * (i - 4));
    const int fg0 = 16 * hf + (typeA ? i : 4 + 3 * (i - 4));
    const int rot = (i + 2 * (slot >> 3)) & 7;
    const int tile64 = 4 * xcd + (slot >> 3), d0 = 64 * (slot & 7);
    const unsigned yrot = (unsigned)(((7 * (slot & 7)) >> 2) + 3 * (slot >> 3)) % 14u;
    if (threadIdx.x < 16) *(LAS unsigned*)(unsigned)(L_SY + threadIdx.x * 4) = 0u;
    u32x4 xv;
    if (w < 4) m_team(x, w2, ws, b, P, typeA, col0 >> 5, fg0, 8 * hf + i, tile64, d0, rot, yrot, w, lane, xv);
    else s_team(x, w1, w2, wr, wi, ws, b, typeA, col0, fg0, tile64, d0, rot, yrot, w - 4, lane, xv);
    WAIT_LGKM0();
    barrier();
    STAMP(14);
    int lane2 = lane;
    asm volatile("" : "+v"(lane2));
    const int n = lane2 & 15, g = lane2 >> 4;
    const int T = w & 3, dp = w >> 2;
    float y[8], s[8];
#pragma unroll
    for (int e = 0; e < 2; ++e) {
        const unsigned a = (2 * dp + (g >> 1)) * 1024 + (n + 16 * (2 * (g & 1) + e)) * 16;
        f32x4 py[4], ps[4];
#pragma unroll
        for (int k = 0; k < 4; ++k) { py[k] = *(LAS f32x4*)(a + (T * 4 + k) * 4096); ps[k] = *(LAS f32x4*)(a + L_Z + k * 16384 + T * 4096); }
#pragma unroll
        for (int r = 0; r < 4; ++r) {
            y[4 * e + r] = (py[0][r] + py[1][r]) + (py[2][r] + py[3][r]);
            s[4 * e + r] = (ps[0][r] + ps[1][r]) + (ps[2][r] + ps[3][r]);
        }
    }
    float o[8];
#pragma unroll
    for (int j = 0; j < 8; ++j) {
        const float yb = bf_round(y[j]);
        const float gg = bf_round(yb * __builtin_amdgcn_rcpf(1.0f + __expf(-yb)));
        const float sb = bf_round(s[j] * (2.0f / 512.0f));
        const float xx = (j & 1) ? bf_hi(xv[j >> 1]) : bf_lo(xv[j >> 1]);
        o[j] = xx + bf_round(sb * gg);
    }
    const u32x4 ov = {pk2(o[0], o[1]), pk2(o[2], o[3]), pk2(o[4], o[5]), pk2(o[6], o[7])};
    __builtin_amdgcn_raw_buffer_store_b128(ov, rsrc(out, 2097152), out_off(tile64, d0, w, lane), 0, 17);
    STAMP(15);
}

extern "C" void kernel_launch(void* const* d_in, const int* in_sizes, int n_in, void* d_out, int out_size, void* d_ws,
                              size_t ws_size, hipStream_t stream) {
    (void)in_sizes; (void)n_in; (void)out_size; (void)ws_size;
    (void)hipFuncSetAttribute((const void*)fused_kernel, hipFuncAttributeMaxDynamicSharedMemorySize, L_END);
    fused_kernel<<<256, 512, L_END, stream>>>((const char*)d_in[0], (const unsigned short*)d_in[1], (const unsigned short*)d_in[2],
                                              (const char*)d_in[4], (const char*)d_in[5], (char*)d_ws, (char*)d_out);
}
```

```cpp
#include <hip/hip_runtime.h>
#include <stdint.h>
#pragma clang diagnostic ignored "-Wint-to-pointer-cast"

typedef short s16x4 __attribute__((ext_vector_type(4)));
typedef __bf16 bf16x8 __attribute__((ext_vector_type(8)));
typedef __bf16 bf16x2 __attribute__((ext_vector_type(2)));
typedef float f32x4 __attribute__((ext_vector_type(4)));
typedef float f32x2 __attribute__((ext_vector_type(2)));
typedef unsigned u32x4 __attribute__((ext_vector_type(4)));
typedef unsigned u32x2 __attribute__((ext_vector_type(2)));
typedef unsigned long long u64;
#define LAS __attribute__((address_space(3)))
#define GAS __attribute__((address_space(1)))
#define DEVI __device__ __forceinline__

constexpr size_t WS_H = 0;
constexpr size_t WS_X1T = 8u << 20;
constexpr size_t WS_WT = 12u << 20;
constexpr size_t WS_ZT = 16u << 20;
constexpr size_t WS_FLAG = 20u << 20;
constexpr size_t WS_TS = 24u << 20;
constexpr unsigned FL_H = 4096, FL_A = 8192, FL_Z = 8192 + 16384;

constexpr unsigned L_W1B = 65536, L_F0 = 131072, L_F1 = 32768;
constexpr unsigned L_CS = 161792, L_ST = L_CS + 512, L_SY = L_ST + 1024, L_END = L_SY + 64;
constexpr unsigned L_Z = 81920, L_BP = 131072, L_ZS = L_BP + 18432, L_WS = 147456;
constexpr unsigned SY_TEAM = L_SY, SY_ZARR = L_SY + 4, SY_ZOK = L_SY + 8, SY_AARR = L_SY + 12, SY_AOK = L_SY + 16, SY_HARR = L_SY + 20,
                   SY_BOK = L_SY + 24, SY_CS2 = L_SY + 28, SY_H47 = L_SY + 32, SY_W2OK = L_SY + 36, SY_Y6 = L_SY + 40, SY_XR = L_SY + 44;
constexpr unsigned SPIN_CAP = 1u << 16;
#define TOUCH 15
constexpr unsigned CODE_PIECES = 11;

#define STAMPC(id) do { } while (0)
#define STAMP4(a, b) do { } while (0)
#define STAMP2(id) do { } while (0)
#define STAMP3(id) do { } while (0)
#define STAMP(id) do { } while (0)

DEVI unsigned pk2(float a, float b) {
    f32x2 f = {a, b};
    return __builtin_bit_cast(unsigned, __builtin_convertvector(f, bf16x2));
}
DEVI float bf_round(float a) { return __builtin_bit_cast(float, pk2(a, a) << 16); }
DEVI float bf_lo(unsigned u) { return __builtin_bit_cast(float, u << 16); }
DEVI float bf_hi(unsigned u) { return __builtin_bit_cast(float, u & 0xffff0000u); }
DEVI f32x4 mfma(u32x4 a, u32x4 b, f32x4 c) {
    return __builtin_amdgcn_mfma_f32_16x16x32_bf16(__builtin_bit_cast(bf16x8, a), __builtin_bit_cast(bf16x8, b), c, 0, 0, 0);
}
DEVI void mfma_ip(f32x4& c, u32x4 a, u32x4 b, bool first) {
    if (first) asm volatile("v_mfma_f32_16x16x32_bf16 %0, %1, %2, 0" : "=v"(c) : "v"(a), "v"(b));
    else asm volatile("v_mfma_f32_16x16x32_bf16 %0, %1, %2, %0" : "+v"(c) : "v"(a), "v"(b));
}
#define MFMA_DONE() asm volatile("s_nop 7\n\ts_nop 1" ::: "memory")
#define WAIT_LGKM(n) __builtin_amdgcn_s_waitcnt(0xC07F | ((n) << 8))
DEVI u32x2 trread(unsigned addr) {
    s16x4 v = __builtin_amdgcn_ds_read_tr16_b64_v4i16((LAS s16x4*)addr);
    return __builtin_bit_cast(u32x2, v);
}
DEVI u32x4 trread2(unsigned a0, unsigned a1) {
    u32x2 lo = trread(a0), hi = trread(a1);
    u32x4 r = {lo[0], lo[1], hi[0], hi[1]};
    return r;
}
DEVI u32x4 lds_rd16(unsigned addr) { return *(LAS u32x4*)addr; }
DEVI void lds_wr16(unsigned addr, u32x4 v) { *(LAS u32x4*)addr = v; }
DEVI void lds_wr8(unsigned addr, u32x2 v) { *(LAS u32x2*)addr = v; }
DEVI float cosr(float rev) { return __builtin_amdgcn_cosf(rev); }
DEVI float sinr(float rev) { return __builtin_amdgcn_sinf(rev); }
DEVI void barrier() { asm volatile("s_barrier" ::: "memory"); }
#define WAIT_VM(n) asm volatile("s_waitcnt vmcnt(%0)" ::"n"(n) : "memory")
#define WAIT_LGKM0() asm volatile("s_waitcnt lgkmcnt(0)" ::: "memory")
#define PIN4(a) asm volatile("" : "+v"(a))
DEVI __amdgpu_buffer_rsrc_t rsrc(char* base, unsigned bytes) { return __builtin_amdgcn_make_buffer_rsrc(base, 0, bytes, 0x00020000); }
DEVI void st_sc1(u32x4 v, __amdgpu_buffer_rsrc_t r, unsigned off) { __builtin_amdgcn_raw_buffer_store_b128(v, r, off, 0, 16); }
DEVI u32x4 ld_sc1(__amdgpu_buffer_rsrc_t r, unsigned off) { return __builtin_amdgcn_raw_buffer_load_b128(r, off, 0, 16); }

DEVI void dma16(unsigned voff, const void* sbase, unsigned dst) {
    unsigned keep;
    asm volatile("s_mov_b32 %0, m0\n\ts_mov_b32 m0, %3\n\ts_nop 0\n\tglobal_load_lds_dwordx4 %1, %2\n\ts_mov_b32 m0, %0"
                 : "=&s"(keep) : "v"(voff), "s"(sbase), "s"(dst) : "memory");
}
template <unsigned IMM>
DEVI void dmaq(unsigned voff, const void* sbase, unsigned sdst) {
    asm volatile("s_add_u32 m0, %2, %3\n\ts_nop 0\n\tglobal_load_lds_dwordx4 %0, %1" :: "v"(voff), "s"(sbase), "s"(sdst), "n"(IMM) : "memory", "scc");
}
DEVI unsigned lds_ld_relaxed(unsigned addr) { return __hip_atomic_load((LAS unsigned*)addr, __ATOMIC_RELAXED, __HIP_MEMORY_SCOPE_WORKGROUP); }
DEVI unsigned lds_arrive(unsigned cnt, int lane) {
    unsigned old = 0;
    if (lane == 0) old = __hip_atomic_fetch_add((LAS unsigned*)cnt, 1u, __ATOMIC_RELAXED, __HIP_MEMORY_SCOPE_WORKGROUP);
    return __builtin_amdgcn_readfirstlane(old);
}
DEVI void lds_wait_ge(unsigned cnt, unsigned target) {
#pragma unroll 1
    for (unsigned s = 0; s < SPIN_CAP * 16; ++s) {
        asm volatile("" ::: "memory");
        if ((unsigned)__builtin_amdgcn_readfirstlane(lds_ld_relaxed(cnt)) >= target) break;
        __builtin_amdgcn_s_sleep(1);
    }
    asm volatile("" ::: "memory");
}
DEVI void team_sync(unsigned cnt, unsigned target, int lane) {
    WAIT_LGKM0();
    if (lane == 0) __hip_atomic_fetch_add((LAS unsigned*)cnt, 1u, __ATOMIC_RELAXED, __HIP_MEMORY_SCOPE_WORKGROUP);
    lds_wait_ge(cnt, target);
}
DEVI u64 epoch_request(const char* ws) { return __hip_atomic_load((const GAS u64*)(ws + WS_FLAG), __ATOMIC_RELAXED, __HIP_MEMORY_SCOPE_AGENT); }
DEVI u64 epoch_finish(u64 e) {
    const u64 v = ((u64)(unsigned)__builtin_amdgcn_readfirstlane((unsigned)(e >> 32)) << 32) | (unsigned)__builtin_amdgcn_readfirstlane((unsigned)e);
    return v + 1;
}
DEVI void post_replicas(char* ws, unsigned off, int b, int lane, u64 epoch) {
    if (lane < 8) __hip_atomic_store((GAS u64*)(ws + WS_FLAG + off + lane * 2048 + b * 8), epoch, __ATOMIC_RELAXED, __HIP_MEMORY_SCOPE_AGENT);
}
template <bool W2>
DEVI void poll256(const char* ws, unsigned off, int b, int lane, u64 epoch) {
    const char* fb = ws + WS_FLAG + off + (b & 7) * 2048;
    const unsigned foff = lane * 16, lo = (unsigned)epoch, hi = (unsigned)(epoch >> 32);
#pragma unroll 1
    for (unsigned s = 0; s < SPIN_CAP; ++s) {
        u32x4 f0, f1;
        asm volatile("global_load_dwordx4 %0, %2, %3 sc1\n\tglobal_load_dwordx4 %1, %2, %3 offset:1024 sc1\n\ts_waitcnt vmcnt(0)"
                     : "=&v"(f0), "=&v"(f1) : "v"(foff), "s"(fb) : "memory");
        const unsigned d = ((f0[0] ^ lo) | (f0[2] ^ lo) | (f1[0] ^ lo) | (f1[2] ^ lo)) | ((f0[1] ^ hi) | (f0[3] ^ hi) | (f1[1] ^ hi) | (f1[3] ^ hi));
        if (W2 && s == 0) (void)lds_arrive(SY_W2OK, lane);
        if (__all(d == 0u)) break;
        __builtin_amdgcn_s_sleep(2);
    }
    asm volatile("" ::: "memory");
}

DEVI unsigned out_off(int tile64, int d0, int w, int lane) {
    asm volatile("" : "+v"(lane));
    const int n = lane & 15, g = lane >> 4, T = w & 3, dp = w >> 2;
    const unsigned tok = 32u * (32 * (T >> 1) + 8 * (n >> 2) + 4 * (T & 1) + (n & 3)) + tile64;
    return tok * 1024u + (d0 + 32 * dp + 8 * g) * 2;
}
DEVI void h_epilogue(const f32x4& a0, const f32x4& a1, unsigned csa, float mb, float rr, __amdgpu_buffer_rsrc_t rh, unsigned off) {
    const f32x4 c0 = *(LAS f32x4*)(csa), c1 = *(LAS f32x4*)(csa + 64);
    float v[8];
#pragma unroll
    for (int r = 0; r < 4; ++r) {
        v[r] = fmaxf(rr * (a0[r] - mb * c0[r]), 0.0f);
        v[4 + r] = fmaxf(rr * (a1[r] - mb * c1[r]), 0.0f);
    }
    const u32x4 o = {pk2(v[0], v[1]), pk2(v[2], v[3]), pk2(v[4], v[5]), pk2(v[6], v[7])};
    st_sc1(o, rh, off);
}

template <int S> struct IC { static constexpr int v = S; };
constexpr int H_RING = 6;
DEVI unsigned y_blk(int i, unsigned yrot) { const unsigned t = yrot + i; return (t >= 14) ? t - 14 : t; }
constexpr int y_slot(int i) { return i < 4 ? i : i < 7 ? i + 1 : i < 12 ? i - 7 : i - 12; }
template <int I>
DEVI void w2_step(const char* wb, unsigned voffw, unsigned sd, unsigned yrot) {
    const char* sb = wb + (size_t)(y_blk(I, yrot) * (128u * 1024u));
    dmaq<y_slot(I) * 16384>(voffw, sb, sd);
    dmaq<y_slot(I) * 16384 + 1024>(voffw + 8192, sb, sd);
    dmaq<y_slot(I) * 16384 + 2048>(voffw + 16384, sb, sd);
    dmaq<y_slot(I) * 16384 + 3072>(voffw + 24576, sb, sd);
}
DEVI void f_gen(int fg, unsigned foff, int rot, int u, int lane) {
    const int n = lane & 15, g = lane >> 4;
    const int c = 16 * fg + n;
    const int k2 = (c == 1) ? 256 : (c >> 1), part = (c == 1) ? 0 : (c & 1);
    const float st = (float)k2 * (1.0f / 512.0f), ph = (float)part * 0.25f;
    const unsigned sg = (k2 & 1) ? 0x80008000u : 0u;
    const float d0 = (float)(64 * u + 8 * g + 64 * rot);
#pragma unroll
    for (int e = 0; e < 2; ++e) {
        const float x0 = __builtin_amdgcn_fractf(st * (d0 + 32.0f * e) + ph);
        float v[8];
#pragma unroll
        for (int j = 0; j < 8; ++j) v[j] = cosr(x0 + (float)j * st);
        const u32x4 o = {pk2(v[0], v[1]), pk2(v[2], v[3]), pk2(v[4], v[5]), pk2(v[6], v[7])};
        const u32x4 o2 = {o[0] ^ sg, o[1] ^ sg, o[2] ^ sg, o[3] ^ sg};
        lds_wr16(foff + (2 * u + e) * 1024 + lane * 16, o);
        lds_wr16(foff + (2 * u + e + 8) * 1024 + lane * 16, o2);
    }
}
template <int NF, int NG>
DEVI void m_first(const char* x, char* ws, u32x4 (&xr)[16][2], float (&mb)[2], float (&rr)[2], __amdgpu_buffer_rsrc_t rh, unsigned hoff,
                  int P, int w, int lane, int fg0, int rot) {
    const int n = lane & 15, g = lane >> 4, q = n >> 2, p = n & 3;
    const int t0 = 2 * P + (w >> 1);
    unsigned vx[4];
    {
        const unsigned r8 = lane >> 3, c8 = lane & 7;
        const unsigned v0 = (32u * (32 * (w & 1) + 8 * (r8 >> 2) + (r8 & 3)) + t0) * 1024u + ((c8 ^ (r8 >> 1)) * 16);
        vx[0] = v0; vx[1] = (v0 ^ 64u) + 524288u; vx[2] = v0 + 131072u; vx[3] = (v0 ^ 64u) + 524288u + 131072u;
    }
    const unsigned sdx = L_W1B + w * 4096;
    auto dmx = [&](auto J) {
        constexpr int j = decltype(J)::v;
        const char* sb = x + (((j + rot) & 7) << 7);
        dmaq<(j & 3) * 16384>(vx[0], sb, sdx); dmaq<(j & 3) * 16384 + 1024>(vx[1], sb, sdx);
        dmaq<(j & 3) * 16384 + 2048>(vx[2], sb, sdx); dmaq<(j & 3) * 16384 + 3072>(vx[3], sb, sdx);
    };
    const unsigned xa0 = sdx + n * 128 + ((g ^ ((n >> 1) & 7)) * 16), xa1 = xa0 ^ 64u;
    auto rdx = [&](int j) {
        xr[2 * j][0] = lds_rd16(xa0 + (j & 3) * 16384); xr[2 * j][1] = lds_rd16(xa0 + (j & 3) * 16384 + 2048);
        xr[2 * j + 1][0] = lds_rd16(xa1 + (j & 3) * 16384); xr[2 * j + 1][1] = lds_rd16(xa1 + (j & 3) * 16384 + 2048);
    };
    dmx(IC<0>{}); dmx(IC<1>{});
    if (NF == 3) { f_gen(fg0 + 1, L_F1, rot, w, lane); f_gen(fg0 + 2, L_F1 + 16384, rot, w, lane); }
    else f_gen(fg0, L_F0, rot, w, lane);
    dmx(IC<2>{}); dmx(IC<3>{});
    STAMP(1);
    WAIT_VM(12); rdx(0); WAIT_LGKM0();
    f32x4 acc[2][NG], af[2][NF], aS[2], aQ[2];
    u32x4 ones = {0x3f803f80u, 0x3f803f80u, 0x3f803f80u, 0x3f803f80u};
    PIN4(ones);
    {
        constexpr unsigned PITCH = (NG == 4) ? 128 : 64;
        const unsigned swz = (NG == 4) ? ((q >> 1) | ((g & 1) << 1)) : (g & 1);
        const unsigned abase = (8 * g + q) * PITCH + swz * 32 + p * 8;
        u32x4 A[NG], F[NF];
        auto rdA = [&](int ks, int db) -> u32x4 { const unsigned a = (abase ^ (db << 5)) + ks * 32 * PITCH; return trread2(a, a + 4 * PITCH); };
        auto rdF = [&](int ks, int f) -> u32x4 { return lds_rd16(((f == 0) ? L_F0 : L_F1 + (f - 1) * 16384) + ks * 1024 + lane * 16); };
        __builtin_amdgcn_s_setprio(2);
        barrier();
        STAMP(2);
#pragma unroll
        for (int db = 0; db < NG; ++db) A[db] = rdA(0, db);
#pragma unroll
        for (int f = 0; f < NF; ++f) F[f] = rdF(0, f);
#pragma unroll
        for (int ks = 0; ks < 16; ++ks) {
            STAMP2(ks);
            if (ks == 0) STAMP4(84, 85); if (ks == 2) STAMP4(86, 87); if (ks == 4) STAMP4(88, 89); if (ks == 6) STAMP4(90, 91);
            if (ks == 8) STAMP4(92, 93); if (ks == 10) STAMP4(94, 95); if (ks == 12) STAMP4(96, 97); if (ks == 14) STAMP4(98, 99);
            if (ks == 1 || ks == 3 || ks == 7) barrier();
            if (ks == 0) dmx(IC<4>{});
            if (ks == 2) { WAIT_LGKM(7); dmx(IC<5>{}); }
            if (ks == 4) { WAIT_LGKM(7); dmx(IC<6>{}); }
            if (ks == 6) { WAIT_LGKM(7); dmx(IC<7>{}); }
            if (ks == 1 || ks == 3 || ks == 5 || ks == 7) { WAIT_VM(12); rdx((ks + 1) / 2); }
            if (ks == 9) { WAIT_VM(8); rdx(5); }
            if (ks == 11) { WAIT_VM(4); rdx(6); }
            if (ks == 13) { WAIT_VM(0); rdx(7); }
            if ((ks == 7 || ks == 9 || ks == 11 || ks == 13) && lane == 0)
                (void)__hip_atomic_fetch_add((LAS unsigned*)SY_XR, 1u, __ATOMIC_RELAXED, __HIP_MEMORY_SCOPE_WORKGROUP);
#pragma unroll
            for (int db = 0; db < 4; ++db) {
                if (db < NG) {
                    mfma_ip(acc[0][db], A[db], xr[ks][0], ks == 0);
                    mfma_ip(acc[1][db], A[db], xr[ks][1], ks == 0);
                    if (ks < 15) A[db] = rdA(ks + 1, db);
                }
                if (db < 2) mfma_ip(aS[db], ones, xr[ks][db], ks == 0);
                else mfma_ip(aQ[db - 2], xr[ks][db - 2], xr[ks][db - 2], ks == 0);
                __builtin_amdgcn_sched_barrier(0);
            }
#pragma unroll
            for (int f = 0; f < NF; ++f) {
                mfma_ip(af[0][f], xr[ks][0], F[f], ks == 0);
                mfma_ip(af[1][f], xr[ks][1], F[f], ks == 0);
                if (ks < 15) F[f] = rdF(ks + 1, f);
                __builtin_amdgcn_sched_barrier(0);
            }
        }
    }
    MFMA_DONE();
#pragma unroll
    for (int tt = 0; tt < 2; ++tt) {
        PIN4(aS[tt]); PIN4(aQ[tt]);
#pragma unroll
        for (int i = 0; i < NG; ++i) PIN4(acc[tt][i]);
#pragma unroll
        for (int f = 0; f < NF; ++f) PIN4(af[tt][f]);
    }
    STAMP(3);
#pragma unroll
    for (int tt = 0; tt < 2; ++tt) {
        const float sq = (p & 2) ? ((p & 1) ? aQ[tt][3] : aQ[tt][2]) : ((p & 1) ? aQ[tt][1] : aQ[tt][0]);
        const float mean = aS[tt][0] * (1.0f / 512.0f);
        const float var = fmaxf(sq * (1.0f / 512.0f) - mean * mean, 0.0f);
        const float m = bf_round(mean);
        const float den = bf_round(__builtin_sqrtf(bf_round(bf_round(var) + 1e-5f)));
        const float r = 1.0f / den;
        if (g == q) lds_wr8(L_ST + (w * 32 + tt * 16 + n) * 8, u32x2{__builtin_bit_cast(unsigned, m), __builtin_bit_cast(unsigned, r)});
    }
    WAIT_LGKM0();
    {
        f32x4 st[2][2];
#pragma unroll
        for (int tt = 0; tt < 2; ++tt) {
            st[tt][0] = *(LAS f32x4*)(L_ST + (w * 32 + tt * 16 + 4 * g) * 8);
            st[tt][1] = *(LAS f32x4*)(L_ST + (w * 32 + tt * 16 + 4 * g) * 8 + 16);
            const f32x2 s = *(LAS f32x2*)(L_ST + (w * 32 + tt * 16 + n) * 8);
            mb[tt] = s[0]; rr[tt] = s[1];
        }
        const __amdgpu_buffer_rsrc_t rx = rsrc(ws + WS_X1T, 512 * 4096);
#pragma unroll
        for (int f = 0; f < NF; ++f) {
            const int c = 16 * (fg0 + f) + n;
            const float cs = (c == 0) ? 512.0f : 0.0f;
            float v[8];
#pragma unroll
            for (int tt = 0; tt < 2; ++tt)
#pragma unroll
                for (int r = 0; r < 4; ++r) v[4 * tt + r] = st[tt][r >> 1][2 * (r & 1) + 1] * (af[tt][f][r] - st[tt][r >> 1][2 * (r & 1)] * cs);
            const u32x4 o = {pk2(v[0], v[1]), pk2(v[2], v[3]), pk2(v[4], v[5]), pk2(v[6], v[7])};
            st_sc1(o, rx, (c >> 1) * 8192 + (4 * P + w) * 128 + (c & 1) * 64 + g * 16);
        }
    }
    STAMP(5);
    __builtin_amdgcn_s_setprio(0);
    barrier();
    STAMP(4);
#pragma unroll
    for (int i2 = 0; i2 < NG / 2; ++i2)
#pragma unroll
        for (int tt = 0; tt < 2; ++tt)
            h_epilogue(acc[tt][2 * i2], acc[tt][2 * i2 + 1], L_CS + (32 * i2 + 4 * g) * 4, mb[tt], rr[tt], rh, hoff + (i2 * 4 + tt) * 1024);
}

DEVI void m_phase_a(const char* x, char* ws, const char* wb, unsigned voffw, int b, int P, bool typeA, int w, int lane, int ii0, int fg0, int hj, int rot, u64& epoch) {
    const int n = lane & 15, g = lane >> 4, q = n >> 2, p = n & 3;
    const u64 ereq = epoch_request(ws);
    u32x4 xr[16][2];
    float mb[2], rr[2];
    const __amdgpu_buffer_rsrc_t rh = rsrc(ws + WS_H, 8u << 20);
    const unsigned hoff = (((2 * P + (w >> 1)) * 56 + ii0) * 4 + 2 * (w & 1)) * 1024 + lane * 16;
    if (typeA) m_first<1, 4>(x, ws, xr, mb, rr, rh, hoff, P, w, lane, fg0, rot);
    else m_first<3, 2>(x, ws, xr, mb, rr, rh, hoff, P, w, lane, fg0, rot);
    if (typeA) WAIT_VM(4); else WAIT_VM(2);
    epoch = epoch_finish(ereq);
    if (lds_arrive(SY_AARR, lane) == 7) post_replicas(ws, FL_A, b, lane, epoch);
    lds_wait_ge(SY_BOK, 4);
    STAMP(6); STAMPC(12);
    f32x4 a2[2][4];
    {
        const unsigned swz = (q >> 1) | ((g & 1) << 1);
        const unsigned abase = L_W1B + (8 * g + q) * 128 + swz * 32 + p * 8;
        u32x2 Al[8], Ah[8];
        auto adr = [&](int j) -> unsigned { return (abase ^ ((j & 3) << 5)) + (j >> 2) * 4096; };
#pragma unroll
        for (int j = 0; j < 7; ++j) { Al[j] = trread(adr(j)); Ah[j] = trread(adr(j) + 512); }
        __builtin_amdgcn_sched_barrier(0);
#pragma unroll
        for (int j = 0; j < 64; ++j) {
            if ((j & 3) == 0) { if (j + 7 < 64) WAIT_LGKM(6); else WAIT_LGKM(0); }
            const u32x4 a = {Al[j & 7][0], Al[j & 7][1], Ah[j & 7][0], Ah[j & 7][1]};
            mfma_ip(a2[0][j & 3], a, xr[j >> 2][0], j < 4);
            __builtin_amdgcn_sched_barrier(0);
            if (j + 7 < 64) Al[(j + 7) & 7] = trread(adr(j + 7));
            __builtin_amdgcn_sched_barrier(0);
            mfma_ip(a2[1][j & 3], a, xr[j >> 2][1], j < 4);
            __builtin_amdgcn_sched_barrier(0);
            if (j + 7 < 64) Ah[(j + 7) & 7] = trread(adr(j + 7) + 512);
            __builtin_amdgcn_sched_barrier(0);
        }
        MFMA_DONE();
#pragma unroll
        for (int db = 0; db < 4; ++db) { PIN4(a2[0][db]); PIN4(a2[1][db]); }
    }
    WAIT_LGKM0();
    (void)lds_arrive(SY_H47, lane);
    STAMP(7); STAMPC(13);
    lds_wait_ge(SY_CS2, 4);
    const unsigned hoff2 = hoff + (typeA ? 8192 : 4096);
#pragma unroll
    for (int i2 = 0; i2 < 2; ++i2)
#pragma unroll
        for (int tt = 0; tt < 2; ++tt)
            h_epilogue(a2[tt][2 * i2], a2[tt][2 * i2 + 1], L_CS + (64 + 32 * i2 + 4 * g) * 4, mb[tt], rr[tt], rh, hoff2 + (i2 * 4 + tt) * 1024);
    WAIT_VM(0);
    if (lds_arrive(SY_HARR, lane) == 3 && lane == 0)
        __hip_atomic_store((GAS u64*)(ws + WS_FLAG + FL_H + P * 128 + hj * 8), epoch, __ATOMIC_RELAXED, __HIP_MEMORY_SCOPE_AGENT);
    STAMP(8);
}

#define GLOAD16(dst, voff, sbase, imm) asm volatile("global_load_dwordx4 %0, %1, %2 offset:%3 sc1" : "=v"(dst) : "v"(voff), "s"(sbase), "n"(imm) : "memory")
constexpr int y_ops(int j) { return ((j == 4 || j == 5) ? 4 : 0) + ((j + H_RING < 14) ? 4 : 0); }
constexpr int y_after(int i) {
    int cnt = (i < H_RING) ? 4 * (H_RING - 1 - i) : 0;
    for (int j = (i < H_RING) ? 0 : (i - (H_RING - 1)); j < i; ++j) cnt += y_ops(j);
    if (i == 8 || i == 9) {
        int c2 = (i - 4 + H_RING < 14) ? 4 : 0;
        for (int j = i - 3; j < i; ++j) c2 += y_ops(j);
        if (c2 < cnt) cnt = c2;
    }
    return cnt;
}

DEVI void m_team(const char* x, const char* __restrict__ w2, char* ws, int b, int P, bool typeA, int ii0, int fg0, int hj,
                 int tile64, int d0, int rot, unsigned yrot, int w, int lane, u32x4& xv) {
    const int n = lane & 15, g = lane >> 4, q = n >> 2, p = n & 3;
    const int r8 = lane >> 3, p16 = lane & 7;
    const unsigned voffw = r8 * 1024 + ((p16 ^ (2 * ((r8 >> 1) & 3))) * 16);
    const char* wb = w2 + d0 * 2 + (size_t)w * 32 * 1024;
    u64 epoch;
    m_phase_a(x, ws, wb, voffw, b, P, typeA, w, lane, ii0, fg0, hj, rot, epoch);

    const unsigned sdw = w * 4096;
    const unsigned abase = w * 4096 + (4 * g + q) * 128 + (((2 * g + (q >> 1)) & 3) * 32) + p * 8;
    u32x4 A[4][4];
    auto rdA = [&](int i) {
#pragma unroll
        for (int db = 0; db < 4; ++db) { const unsigned a = (abase ^ (db << 5)) + y_slot(i) * 16384; A[i & 3][db] = trread2(a, a + 2048); }
    };
    lds_wait_ge(SY_W2OK, 4);
    rdA(0); rdA(1); rdA(2); rdA(3);
    lds_wait_ge(SY_H47, 4);
    w2_step<4>(wb, voffw, sdw, yrot); w2_step<5>(wb, voffw, sdw, yrot); w2_step<6>(wb, voffw, sdw, yrot); w2_step<11>(wb, voffw, sdw, yrot);
#pragma unroll
    for (int i = 0; i < 4; ++i)
#pragma unroll
        for (int db = 0; db < 4; ++db) PIN4(A[i][db]);
    WAIT_LGKM0();
    w2_step<7>(wb, voffw, sdw, yrot); w2_step<8>(wb, voffw, sdw, yrot); w2_step<9>(wb, voffw, sdw, yrot); w2_step<10>(wb, voffw, sdw, yrot);
    {
        const char* fb = ws + WS_FLAG + FL_H + (tile64 >> 1) * 128;
        const unsigned foff = (lane & 15) * 8, lo = (unsigned)epoch, hi = (unsigned)(epoch >> 32);
#pragma unroll 1
        for (unsigned s = 0; s < SPIN_CAP; ++s) {
            u32x2 f;
            asm volatile("global_load_dwordx2 %0, %1, %2 sc1\n\ts_waitcnt vmcnt(0)" : "=v"(f) : "v"(foff), "s"(fb) : "memory");
            if (__all(((f[0] ^ lo) | (f[1] ^ hi)) == 0u)) break;
            __builtin_amdgcn_s_sleep(2);
        }
        asm volatile("" ::: "memory");
    }
    STAMP(9); STAMP3(15);
    const char* hb = ws + WS_H + (size_t)(tile64 * 56 + w) * 4096;
    const unsigned voffh = lane * 16;
    u32x4 h[H_RING][4];
    auto issue_h = [&](int i) {
        const char* hp = hb + (size_t)(y_blk(i, yrot) * 16384u);
        GLOAD16(h[i % H_RING][0], voffh, hp, 0); GLOAD16(h[i % H_RING][1], voffh, hp, 1024);
        GLOAD16(h[i % H_RING][2], voffh, hp, 2048); GLOAD16(h[i % H_RING][3], voffh, hp, 3072);
    };
#pragma unroll
    for (int i = 0; i < H_RING; ++i) issue_h(i);
    f32x4 ay[4][4];
#define T1_WAIT(N, slot) asm volatile("s_waitcnt vmcnt(%4)" : "+v"(h[slot][0]), "+v"(h[slot][1]), "+v"(h[slot][2]), "+v"(h[slot][3]) : "n"(N) : "memory")
    auto ystep = [&](auto I) {
        constexpr int i = decltype(I)::v;
        T1_WAIT(y_after(i), i % H_RING);
        if (i == 0) STAMP(10);
        STAMP3(i);
#pragma unroll
        for (int db = 0; db < 4; ++db)
#pragma unroll
            for (int t = 0; t < 4; ++t) mfma_ip(ay[db][t], A[i & 3][db], h[i % H_RING][t], i == 0);
        __builtin_amdgcn_sched_barrier(0);
        if (i + 4 < 14) rdA(i + 4);
        if (i == 2) (void)lds_arrive(SY_Y6, lane);
        if constexpr (i == 4 || i == 5) { WAIT_LGKM0(); w2_step<i + 8>(wb, voffw, sdw, yrot); }
        if (i + H_RING < 14) issue_h(i + H_RING);
        if (i == 10) xv = *(const GAS u32x4*)((const GAS char*)x + out_off(tile64, d0, w, lane));
        __builtin_amdgcn_sched_barrier(0);
    };
    ystep(IC<0>{}); ystep(IC<1>{}); ystep(IC<2>{}); ystep(IC<3>{}); ystep(IC<4>{}); ystep(IC<5>{}); ystep(IC<6>{});
    ystep(IC<7>{}); ystep(IC<8>{}); ystep(IC<9>{}); ystep(IC<10>{}); ystep(IC<11>{}); ystep(IC<12>{}); ystep(IC<13>{});
    MFMA_DONE();
#pragma unroll
    for (int t = 0; t < 4; ++t)
#pragma unroll
        for (int db = 0; db < 4; ++db) PIN4(ay[db][t]);
    STAMP(11); STAMP3(14);
#pragma unroll
    for (int t = 0; t < 4; ++t)
#pragma unroll
        for (int db = 0; db < 4; ++db) *(LAS f32x4*)((t * 4 + w) * 4096 + db * 1024 + lane * 16) = ay[db][t];
}

template <bool HAS_I, bool FIRST>
DEVI void p2_unit(char* ws, const u32x4 (&Ur)[2][2], const u32x4 (&Ui)[2][2], const u32x4 (&Wf)[2], int zrow, int v, int lane, unsigned& sync_n,
                  const u32x4 (&C64)[2], const u32x4 (&S64)[2], const u32x4 (&N64)[2],
                  const u32x4 (&C32)[2], const u32x4 (&S32)[2], const u32x4 (&N32)[2],
                  const float (&tc)[8], const float (&tsn)[8]) {
    const int n = lane & 15, g = lane >> 4, q = n >> 2, p = n & 3;
    STAMP3(2);
    float br[8], bi[8];
#pragma unroll
    for (int mt = 0; mt < 2; ++mt) {
        f32x4 dr = {0, 0, 0, 0}, di = {0, 0, 0, 0};
#pragma unroll
        for (int ks = 0; ks < 2; ++ks) {
            dr = mfma(Ur[mt][ks], C64[ks], dr);
            di = mfma(Ur[mt][ks], N64[ks], di);
            if (HAS_I) { dr = mfma(Ui[mt][ks], S64[ks], dr); di = mfma(Ui[mt][ks], C64[ks], di); }
        }
#pragma unroll
        for (int r = 0; r < 4; ++r) {
            const float c = tc[4 * mt + r], s = tsn[4 * mt + r];
            br[4 * mt + r] = dr[r] * c + di[r] * s;
            bi[4 * mt + r] = di[r] * c - dr[r] * s;
        }
    }
    u32x4 Br = {pk2(br[0], br[1]), pk2(br[2], br[3]), pk2(br[4], br[5]), pk2(br[6], br[7])};
    u32x4 Bi = {pk2(bi[0], bi[1]), pk2(bi[2], bi[3]), pk2(bi[4], bi[5]), pk2(bi[6], bi[7])};
    float yr[8], yi[8];
#pragma unroll
    for (int t = 0; t < 2; ++t) {
        f32x4 dr = {0, 0, 0, 0}, di = {0, 0, 0, 0};
        dr = mfma(C32[t], Br, dr); dr = mfma(S32[t], Bi, dr);
        di = mfma(C32[t], Bi, di); di = mfma(N32[t], Br, di);
#pragma unroll
        for (int r = 0; r < 4; ++r) {
            const float wre = bf_lo(Wf[t][r]), wim = bf_hi(Wf[t][r]);
            yr[4 * t + r] = dr[r] * wre - di[r] * wim;
            yi[4 * t + r] = dr[r] * wim + di[r] * wre;
        }
    }
    u32x4 Yr = {pk2(yr[0], yr[1]), pk2(yr[2], yr[3]), pk2(yr[4], yr[5]), pk2(yr[6], yr[7])};
    u32x4 Yi = {pk2(yi[0], yi[1]), pk2(yi[2], yi[3]), pk2(yi[4], yi[5]), pk2(yi[6], yi[7])};
#pragma unroll
    for (int mt = 0; mt < 2; ++mt) {
        f32x4 dr = {0, 0, 0, 0}, di = {0, 0, 0, 0};
        dr = mfma(C32[mt], Yr, dr); dr = mfma(N32[mt], Yi, dr);
        di = mfma(C32[mt], Yi, di); di = mfma(S32[mt], Yr, di);
        float pr[4], pi[4];
#pragma unroll
        for (int r = 0; r < 4; ++r) {
            const float c = tc[4 * mt + r] * (1.0f / 2048.0f), s = tsn[4 * mt + r] * (1.0f / 2048.0f);
            pr[r] = dr[r] * c - di[r] * s;
            pi[r] = di[r] * c + dr[r] * s;
        }
        const unsigned a = L_BP + (16 * v + n) * 72 + (16 * mt + 4 * g) * 2;
        lds_wr8(a, u32x2{pk2(pr[0], pr[1]), pk2(pr[2], pr[3])});
        lds_wr8(a + 64 * 72, u32x2{pk2(pi[0], pi[1]), pk2(pi[2], pi[3])});
    }
    STAMP3(3);
    sync_n += 4; team_sync(SY_TEAM, sync_n, lane);
    STAMP3(4);
#pragma unroll
    for (int nt = 0; nt < 2; ++nt) {
        f32x4 dr = {0, 0, 0, 0}, di = {0, 0, 0, 0};
#pragma unroll
        for (int ks = 0; ks < 2; ++ks) {
            const unsigned a = L_BP + (32 * ks + 8 * g + q) * 72 + nt * 32 + p * 8;
            const u32x4 pr = trread2(a, a + 4 * 72), pi = trread2(a + 64 * 72, a + 64 * 72 + 4 * 72);
            dr = mfma(C64[ks], pr, dr); dr = mfma(N64[ks], pi, dr);
            if (HAS_I) { di = mfma(C64[ks], pi, di); di = mfma(S64[ks], pr, di); }
        }
        const unsigned a = L_ZS + (16 * nt + n) * 144 + (16 * v + 4 * g) * 2;
        lds_wr8(a, u32x2{pk2(dr[0], dr[1]), pk2(dr[2], dr[3])});
        if (HAS_I) lds_wr8(a + 32 * 144, u32x2{pk2(di[0], di[1]), pk2(di[2], di[3])});
    }
    STAMP3(5);
    sync_n += 4; team_sync(SY_TEAM, sync_n, lane);
    STAMP3(6);
    {
        const __amdgpu_buffer_rsrc_t rs = rsrc(ws + WS_ZT, 512 * 4096);
#pragma unroll
        for (int it = 0; it < (HAS_I ? 2 : 1); ++it) {
            const int row = 8 * v + (lane >> 3), ch = lane & 7;
            const u32x4 o = lds_rd16(L_ZS + (it * 32 + row) * 144 + ch * 16);
            st_sc1(o, rs, (zrow + it) * 4096 + row * 128 + ch * 16);
        }
    }
}

#define GLOAD_U16(dst, voff, sbase, imm) asm volatile("global_load_ushort %0, %1, %2 offset:%3" : "=v"(dst) : "v"(voff), "s"(sbase), "n"(imm) : "memory")
DEVI void g_gen(u32x4 (&G)[4], int dbase, int v, int lane) {
    const int n = lane & 15, g = lane >> 4;
    const int d = dbase + n;
    float c[4], s[4];
#pragma unroll
    for (int jj = 0; jj < 4; ++jj) {
        const float a = (float)(((64 * v + 4 * g + jj) * d) & 511) * (1.0f / 512.0f);
        c[jj] = cosr(a); s[jj] = sinr(a);
    }
    const float ar = (float)((16 * d) & 511) * (1.0f / 512.0f);
    const float cr = cosr(ar), sr = sinr(ar);
#pragma unroll
    for (int ks = 0; ks < 4; ++ks) {
        u32x4 o;
#pragma unroll
        for (int jj = 0; jj < 4; ++jj) o[jj] = pk2(c[jj], -s[jj]);
        if (ks == 0 && v == 0 && g == 0) o[0] = pk2(0.5f, (d & 1) ? -0.5f : 0.5f);
        G[ks] = o;
        PIN4(G[ks]);
        if (ks < 3) {
#pragma unroll
            for (int jj = 0; jj < 4; ++jj) {
                const float c2 = c[jj] * cr - s[jj] * sr, s2 = s[jj] * cr + c[jj] * sr;
                c[jj] = c2; s[jj] = s2;
            }
        }
    }
}
DEVI void s_team(const char* x, const char* __restrict__ w1, const char* __restrict__ w2, const unsigned short* wr, const unsigned short* wi, char* ws, const char* out, int b,
                 bool typeA, int col0, int fg0, int tile64, int d0, int rot, unsigned yrot, int u, int lane, u32x4& xv) {
    const int n = lane & 15, g = lane >> 4, q = n >> 2, p = n & 3;
    const u64 ereq = epoch_request(ws);
    unsigned pf0 = 0;
    const char* wb = w1 + col0 * 2;
    const int r8 = lane >> 3, p16 = lane & 7;
    const unsigned v128 = r8 * 3584 + (p16 & 1) * 16;
    unsigned va[2];
    if (typeA) {
#pragma unroll
        for (int j = 0; j < 2; ++j) va[j] = v128 + (((p16 >> 1) ^ (((r8 >> 1) & 1) | (j << 1))) * 32) + (2 * u + j) * 8 * 3584;
    } else {
        const int r16 = lane >> 2, c4 = lane & 3;
        va[0] = va[1] = r16 * 3584 + (((c4 >> 1) ^ (r16 >> 3)) * 32) + (c4 & 1) * 16 + u * 16 * 3584;
    }
    const unsigned sdA = typeA ? u * 2048 : u * 1024;
    auto stage_a = [&](auto S) {
        constexpr int s = decltype(S)::v;
        const char* sb = wb + (size_t)(((s + rot) & 7) * (64 * 3584));
        if (typeA) { dmaq<s * 8192>(va[0], sb, sdA); dmaq<s * 8192 + 1024>(va[1], sb, sdA); }
        else dmaq<s * 4096>(va[0], sb, sdA);
    };
    const int rs = b & 63, cq = b >> 6;
    const int jv = rs & 3, ja = (rs >> 2) & 1, jt = (rs >> 3) & 1, jg = rs >> 4;
    unsigned wv[2][4][2], wsp;
    auto filter_loads = [&]() {
        const unsigned k0 = 16 * jv + 8 * ja + 2 * u + 64 * (16 * jt + 4 * jg);
        const unsigned off0 = (k0 * 257 + 64 * cq + lane) * 2;
#pragma unroll
        for (int r = 0; r < 4; ++r) {
            const unsigned off = off0 + r * (64 * 514);
            GLOAD_U16(wv[0][r][0], off, wr, 0); GLOAD_U16(wv[0][r][1], off, wi, 0);
            GLOAD_U16(wv[1][r][0], off, wr, 514); GLOAD_U16(wv[1][r][1], off, wi, 514);
        }
        const int unit = 2 * b + ((lane >> 3) & 1), reg = (lane >> 1) & 3;
        const unsigned k1 = 16 * (unit >> 7) + (unit & 15) + 64 * (16 * ((unit >> 6) & 1) + 4 * ((unit >> 4) & 3) + reg);
        const unsigned long long a = (unsigned long long)((lane & 1) ? wi : wr) + (k1 * 257 + 256) * 2;
        asm volatile("global_load_ushort %0, %1, off" : "=v"(wsp) : "v"(a) : "memory");
    };
    const char* wb2 = wb + (typeA ? 128 : 64);
    const unsigned vbb = v128 + (((p16 >> 1) ^ (((r8 >> 1) & 1) | ((u & 1) << 1))) * 32) + u * 8 * 3584;
    const unsigned sdB = L_W1B + u * 1024;
    auto stage_b = [&](auto K) {
        constexpr int k = decltype(K)::v;
        dmaq<k * 4096>(vbb, wb2 + (size_t)(((k + 2 * rot) & 15) * (32 * 3584)), sdB);
    };
    u32x4 C64[2], S64[2], N64[2], C32[2], S32[2], N32[2];
    float tc[8], tsn[8];
    auto gen_c64 = [&]() {
        const int m = 16 * u + n;
        const float st = (float)m * (1.0f / 64.0f), a0 = __builtin_amdgcn_fractf(st * (float)(8 * g));
        float c[8], sn[8];
#pragma unroll
        for (int j = 0; j < 8; ++j) { const float a = a0 + (float)j * st; c[j] = cosr(a); sn[j] = sinr(a); }
        const unsigned sg = (m & 1) ? 0x80008000u : 0u;
#pragma unroll
        for (int e = 0; e < 4; ++e) {
            C64[0][e] = pk2(c[2 * e], c[2 * e + 1]); S64[0][e] = pk2(sn[2 * e], sn[2 * e + 1]); N64[0][e] = S64[0][e] ^ 0x80008000u;
            C64[1][e] = C64[0][e] ^ sg; S64[1][e] = S64[0][e] ^ sg; N64[1][e] = N64[0][e] ^ sg;
        }
#pragma unroll
        for (int e = 0; e < 2; ++e) { PIN4(C64[e]); PIN4(S64[e]); PIN4(N64[e]); }
    };
    auto gen_c32 = [&]() {
        const float st = (float)n * (1.0f / 32.0f), a0 = st * (float)(4 * g);
        float c[4], sn[4];
#pragma unroll
        for (int j = 0; j < 4; ++j) { const float a = a0 + (float)j * st; c[j] = cosr(a); sn[j] = sinr(a); }
        const unsigned sg = (n & 1) ? 0x80008000u : 0u;
#pragma unroll
        for (int e = 0; e < 2; ++e) {
            C32[0][e] = pk2(c[2 * e], c[2 * e + 1]); S32[0][e] = pk2(sn[2 * e], sn[2 * e + 1]);
            C32[0][e + 2] = C32[0][e] ^ sg; S32[0][e + 2] = S32[0][e] ^ sg;
        }
#pragma unroll
        for (int e = 0; e < 4; ++e) {
            C32[1][e] = C32[0][e] ^ 0x80000000u; S32[1][e] = S32[0][e] ^ 0x80000000u;
            N32[0][e] = S32[0][e] ^ 0x80008000u; N32[1][e] = S32[0][e] ^ 0x00008000u;
        }
#pragma unroll
        for (int e = 0; e < 2; ++e) { PIN4(C32[e]); PIN4(S32[e]); PIN4(N32[e]); }
    };
    auto gen_tw = [&]() {
        const float st = (float)(16 * u + n) * (1.0f / 2048.0f), a0 = st * (float)(4 * g);
#pragma unroll
        for (int j = 0; j < 8; ++j) { const float a = a0 + (float)(16 * (j >> 2) + (j & 3)) * st; tc[j] = cosr(a); tsn[j] = sinr(a); }
#pragma unroll
        for (int j = 0; j < 8; ++j) { PIN4(tc[j]); PIN4(tsn[j]); }
    };
    const u32x4 ones = {0x3f803f80u, 0x3f803f80u, 0x3f803f80u, 0x3f803f80u};
    {
        const unsigned swz = (q >> 1) | ((g & 1) << 1);
        const unsigned abase = typeA ? (((8 * g + q) * 128 + swz * 32 + p * 8) ^ (u << 5)) : ((8 * g + q) * 64 + (((u ^ g) & 1) * 32) + p * 8);
        const unsigned kpitch = typeA ? 4096 : 2048, hpitch = typeA ? 512 : 256;
        f32x4 cs = {0, 0, 0, 0};
        auto col_sums = [&](int s) {
            const unsigned a0 = abase + (2 * s) * kpitch, a1 = a0 + kpitch;
            const u32x4 c0 = trread2(a0, a0 + hpitch), c1 = trread2(a1, a1 + hpitch);
            cs = mfma(c0, ones, cs);
            cs = mfma(c1, ones, cs);
        };
        stage_a(IC<0>{}); stage_a(IC<1>{});
        if (!typeA) f_gen(fg0, L_F0, rot, u, lane);
        stage_a(IC<2>{}); stage_a(IC<3>{});
        STAMP(1);
        if (typeA) WAIT_VM(6); else WAIT_VM(3);
        WAIT_LGKM0();
        STAMP(2);
        barrier();
        STAMP(3);
        filter_loads();
        stage_a(IC<4>{}); stage_a(IC<5>{});
        col_sums(0);
        if (typeA) WAIT_VM(25); else WAIT_VM(21);
        barrier();
        stage_a(IC<6>{}); stage_a(IC<7>{});
        col_sums(1);
        if (typeA) WAIT_VM(25); else WAIT_VM(21);
        barrier();
        col_sums(2); col_sums(3);
        WAIT_VM(0);
        asm volatile("" : "+v"(wv[0][0][0]), "+v"(wv[0][0][1]), "+v"(wv[0][1][0]), "+v"(wv[0][1][1]), "+v"(wv[0][2][0]), "+v"(wv[0][2][1]), "+v"(wv[0][3][0]), "+v"(wv[0][3][1]),
                          "+v"(wv[1][0][0]), "+v"(wv[1][0][1]), "+v"(wv[1][1][0]), "+v"(wv[1][1][1]), "+v"(wv[1][2][0]), "+v"(wv[1][2][1]), "+v"(wv[1][3][0]), "+v"(wv[1][3][1]), "+v"(wsp));
#pragma unroll
        for (int e = 0; e < 2; ++e) {
            u32x4 o;
#pragma unroll
            for (int r = 0; r < 4; ++r) o[r] = wv[e][r][0] | (wv[e][r][1] << 16);
            lds_wr16(L_WS + lane * 144 + (2 * u + e) * 16, o);
        }
        WAIT_LGKM0();
        barrier();
        {
            const __amdgpu_buffer_rsrc_t rw = rsrc(ws + WS_WT, 257 * 8192);
#pragma unroll
            for (int e = 0; e < 2; ++e) {
                const int k2l = 16 * u + 8 * e + (lane >> 3);
                const u32x4 o = lds_rd16(L_WS + k2l * 144 + (lane & 7) * 16);
                st_sc1(o, rw, (64 * cq + k2l) * 8192 + ((jv * 2 + jt) * 64 + 16 * jg + 8 * ja + (lane & 7)) * 16);
            }
            if (u == 0 && lane < 16) __builtin_amdgcn_raw_buffer_store_b16((unsigned short)wsp, rw, 256 * 8192 + 2 * b * 16 + lane * 2, 0, 16);
        }
        col_sums(4); col_sums(5); col_sums(6); col_sums(7);
        if (n == 0 && (typeA || u < 2)) *(LAS f32x4*)(L_CS + (16 * u + 4 * g) * 4) = cs;
        {
            const char* tb = u == 0 ? ws + WS_H + (size_t)tile64 * 56 * 4096 : u == 1 ? w2 : u == 2 ? out : ws + WS_ZT;
            const unsigned to = lane * (u == 0 ? 3584u : u == 1 ? 28672u : 32768u);
            if (TOUCH & (1 << u)) asm volatile("global_load_dword %0, %1, %2" : "=&v"(pf0) : "v"(to), "s"(tb) : "memory");
        }
        lds_wait_ge(SY_XR, 4);  stage_b(IC<0>{});  stage_b(IC<1>{});  stage_b(IC<2>{});  stage_b(IC<3>{});
        lds_wait_ge(SY_XR, 8);  stage_b(IC<4>{});  stage_b(IC<5>{});  stage_b(IC<6>{});  stage_b(IC<7>{});
        lds_wait_ge(SY_XR, 12); stage_b(IC<8>{});  stage_b(IC<9>{});  stage_b(IC<10>{}); stage_b(IC<11>{});
        lds_wait_ge(SY_XR, 16); stage_b(IC<12>{}); stage_b(IC<13>{}); stage_b(IC<14>{}); stage_b(IC<15>{});
    }
    WAIT_LGKM0();
    barrier();
    STAMP(4);
    asm volatile("s_waitcnt vmcnt(0) ; touch %0" : "+v"(pf0) :: "memory");
    (void)lds_arrive(SY_BOK, lane);
    const u64 epoch = epoch_finish(ereq);
    if (lds_arrive(SY_AARR, lane) == 7) post_replicas(ws, FL_A, b, lane, epoch);
    lds_wait_ge(SY_BOK, 4);
    STAMP(5);
    {
        f32x4 cs = {0, 0, 0, 0};
        const unsigned swz = (q >> 1) | ((g & 1) << 1);
        const unsigned abase = L_W1B + (((8 * g + q) * 128 + swz * 32 + p * 8) ^ (u << 5));
#pragma unroll
        for (int ks = 0; ks < 16; ++ks) { const unsigned a = abase + ks * 4096; cs = mfma(trread2(a, a + 512), ones, cs); }
        if (n == 0) *(LAS f32x4*)(L_CS + (64 + 16 * u + 4 * g) * 4) = cs;
    }
    WAIT_LGKM0();
    (void)lds_arrive(SY_CS2, lane);
    {
        const unsigned voffw = r8 * 1024 + ((p16 ^ (2 * ((r8 >> 1) & 3))) * 16);
        const char* wb3 = w2 + d0 * 2 + (size_t)u * 32 * 1024;
        const unsigned sd = u * 4096;
        w2_step<0>(wb3, voffw, sd, yrot); w2_step<1>(wb3, voffw, sd, yrot); w2_step<2>(wb3, voffw, sd, yrot); w2_step<3>(wb3, voffw, sd, yrot);
    }
    gen_tw(); gen_c64(); gen_c32();
    if (u != 0) {
        WAIT_VM(0);
        (void)lds_arrive(SY_W2OK, lane);
    }
    STAMP(6);
    __builtin_amdgcn_s_setprio(1);

    const int v = u;
    if (v == 0) {
        poll256<true>(ws, FL_A, b, lane, epoch);
        if (lane == 0) __hip_atomic_store((LAS unsigned*)SY_AOK, 1u, __ATOMIC_RELAXED, __HIP_MEMORY_SCOPE_WORKGROUP);
    } else lds_wait_ge(SY_AOK, 1u);
    STAMP(7); STAMP3(1);
    u32x4 Ur[2][2], Ui[2][2], Wf[2], Wg[2];
    unsigned sync_n = 0;
    {
        const char* xb = ws + WS_X1T + (size_t)(b * 8192 + v * 2048);
        const char* fb = ws + WS_WT + (size_t)(b * 8192 + v * 2048);
        const char* fb2 = ws + WS_WT + (size_t)(256 * 8192 + v * 2048);
        const unsigned vo = lane * 16;
        u32x4 u0, u1;
        GLOAD16(u0, vo, xb, 0); GLOAD16(u1, vo, xb, 1024);
        GLOAD16(Wf[0], vo, fb, 0); GLOAD16(Wf[1], vo, fb, 1024);
        if (b == 0) { GLOAD16(Wg[0], vo, fb2, 0); GLOAD16(Wg[1], vo, fb2, 1024); }
        else { Wg[0] = u32x4{0, 0, 0, 0}; Wg[1] = u32x4{0, 0, 0, 0}; }
        asm volatile("s_waitcnt vmcnt(0)" : "+v"(u0), "+v"(u1), "+v"(Wf[0]), "+v"(Wf[1]), "+v"(Wg[0]), "+v"(Wg[1]) :: "memory");
        const unsigned ta = 8 * v + g, tb = ta + 4;
        lds_wr16(L_ZS + ta * 256 + ((n ^ ta) & 15) * 16, u0);
        lds_wr16(L_ZS + tb * 256 + ((n ^ tb) & 15) * 16, u1);
        sync_n += 4; team_sync(SY_TEAM, sync_n, lane);
#pragma unroll
        for (int mt = 0; mt < 2; ++mt)
#pragma unroll
            for (int ks = 0; ks < 2; ++ks) {
                const unsigned a = L_ZS + (16 * mt + n) * 256;
                Ur[mt][ks] = lds_rd16(a + ((8 * ks + g) ^ n) * 16);
                Ui[mt][ks] = lds_rd16(a + ((8 * ks + 4 + g) ^ n) * 16);
            }
    }
    if (b != 0) p2_unit<true, true>(ws, Ur, Ui, Wf, 2 * b, v, lane, sync_n, C64, S64, N64, C32, S32, N32, tc, tsn);
    else {
        p2_unit<false, true>(ws, Ur, Ur, Wf, 0, v, lane, sync_n, C64, S64, N64, C32, S32, N32, tc, tsn);
        p2_unit<false, false>(ws, Ui, Ui, Wg, 1, v, lane, sync_n, C64, S64, N64, C32, S32, N32, tc, tsn);
    }
    STAMP(8); STAMP3(7);
    WAIT_VM(0);
    if (lds_arrive(SY_ZARR, lane) == 3) post_replicas(ws, FL_Z, b, lane, epoch);
    STAMP(9); STAMP3(9);
    xv = *(const GAS u32x4*)((const GAS char*)x + out_off(tile64, d0, u + 4, lane));
    u32x4 G[4][4];
    g_gen(G[0], d0, u, lane); g_gen(G[1], d0 + 16, u, lane); g_gen(G[2], d0 + 32, u, lane); g_gen(G[3], d0 + 48, u, lane);
    STAMP(10); STAMP3(10);
    if (v == 0) {
        poll256<false>(ws, FL_Z, b, lane, epoch);
        if (lane == 0) {
            __hip_atomic_store((LAS unsigned*)SY_ZOK, 1u, __ATOMIC_RELAXED, __HIP_MEMORY_SCOPE_WORKGROUP);
            if (b == 0) __hip_atomic_store((GAS u64*)(ws + WS_FLAG), epoch, __ATOMIC_RELAXED, __HIP_MEMORY_SCOPE_AGENT);
        }
    } else lds_wait_ge(SY_ZOK, 1u);
    lds_wait_ge(SY_Y6, 4);
    STAMP(11); STAMP3(11);
    const unsigned zreg = L_Z + v * 16384;
    {
        const __amdgpu_buffer_rsrc_t rz = rsrc(ws + WS_ZT, 512 * 4096);
        const int r8 = lane >> 3, ch = lane & 7;
        u32x4 z[16];
#pragma unroll
        for (int i = 0; i < 16; ++i) z[i] = ld_sc1(rz, (128 * v + 8 * i + r8) * 4096 + tile64 * 128 + ch * 16);
#pragma unroll
        for (int i = 0; i < 16; ++i) lds_wr16(zreg + (8 * i + r8) * 128 + ((ch ^ (4 * ((r8 >> 1) & 1))) * 16), z[i]);
    }
    STAMP(12); STAMP3(12);
    f32x4 acc[4][4];
    {
        auto rdZ = [&](int j) -> u32x4 {
            const int ks = j >> 2, t = j & 3;
            const unsigned a = zreg + (32 * ks + 8 * g + q) * 128 + (((t >> 1) ^ ((q >> 1) & 1)) * 64) + p * 16 + (t & 1) * 8;
            return trread2(a, a + 512);
        };
        u32x4 z[4];
#pragma unroll
        for (int j = 0; j < 4; ++j) z[j] = rdZ(j);
        __builtin_amdgcn_sched_barrier(0);
#pragma unroll
        for (int j = 0; j < 16; ++j) {
#pragma unroll
            for (int db = 0; db < 4; ++db) mfma_ip(acc[db][j & 3], G[db][j >> 2], z[j & 3], j < 4);
            if (j + 4 < 16) z[j & 3] = rdZ(j + 4);
            __builtin_amdgcn_sched_barrier(0);
        }
    }
    MFMA_DONE();
#pragma unroll
    for (int t = 0; t < 4; ++t)
#pragma unroll
        for (int db = 0; db < 4; ++db) PIN4(acc[db][t]);
#pragma unroll
    for (int t = 0; t < 4; ++t)
#pragma unroll
        for (int db = 0; db < 4; ++db) *(LAS f32x4*)(zreg + (t * 4 + db) * 1024 + lane * 16) = acc[db][t];
    __builtin_amdgcn_s_setprio(0);
    STAMP(13); STAMP3(13);
}

#if defined(__HIP_DEVICE_COMPILE__)
#define NOPK __attribute__((target("no-packed-fp32-ops")))
#else
#define NOPK
#endif
__global__ NOPK void __launch_bounds__(512, 2) fused_kernel(const char* x, const unsigned short* wr, const unsigned short* wi,
                                                        const char* __restrict__ w1, const char* __restrict__ w2, char* ws,
                                                        char* __restrict__ out) {
    const int b = blockIdx.x, lane = threadIdx.x & 63, w = __builtin_amdgcn_readfirstlane(threadIdx.x >> 6);
    STAMP(0);
    if (w >= 4) STAMP3(0);
    const int xcd = b & 7, slot = b >> 3;
    if (w == 7 || w == 3) {
        unsigned long long pc;
        asm volatile("s_getpc_b64 %0" : "=s"(pc));
        const unsigned piece = (unsigned)(2 * slot + (w >> 2)) % CODE_PIECES;
        const unsigned voff = piece * 4096 + lane * 64;
        asm volatile("s_mov_b32 m0, %2\n\ts_nop 0\n\tglobal_load_lds_dword %0, %1" :: "v"(voff), "s"(pc), "s"(L_CS) : "memory");
    }
    const int hf = xcd & 1, P = 4 * (xcd >> 1) + (slot >> 3), i = slot & 7;
    const bool typeA = i < 4;
    const int col0 = 896 * hf + (typeA ? 128 * i : 512 + 96 * (i - 4));
    const int fg0 = 16 * hf + (typeA ? i : 4 + 3 * (i - 4));
    const int rot = (i + 2 * (slot >> 3)) & 7;
    const int tile64 = 4 * xcd + (slot >> 3), d0 = 64 * (slot & 7);
    const unsigned yrot = (unsigned)(((7 * (slot & 7)) >> 2) + 3 * (slot >> 3)) % 14u;
    if (threadIdx.x < 16) *(LAS unsigned*)(unsigned)(L_SY + threadIdx.x * 4) = 0u;
    u32x4 xv;
    if (w < 4) m_team(x, w2, ws, b, P, typeA, col0 >> 5, fg0, 8 * hf + i, tile64, d0, rot, yrot, w, lane, xv);
    else s_team(x, w1, w2, wr, wi, ws, out, b, typeA, col0, fg0, tile64, d0, rot, yrot, w - 4, lane, xv);
    WAIT_LGKM0();
    barrier();
    STAMP(14);
    int lane2 = lane;
    asm volatile("" : "+v"(lane2));
    const int n = lane2 & 15, g = lane2 >> 4;
    const int T = w & 3, dp = w >> 2;
    float y[8], s[8];
#pragma unroll
    for (int e = 0; e < 2; ++e) {
        const unsigned a = (2 * dp + (g >> 1)) * 1024 + (n + 16 * (2 * (g & 1) + e)) * 16;
        f32x4 py[4], ps[4];
#pragma unroll
        for (int k = 0; k < 4; ++k) { py[k] = *(LAS f32x4*)(a + (T * 4 + k) * 4096); ps[k] = *(LAS f32x4*)(a + L_Z + k * 16384 + T * 4096); }
#pragma unroll
        for (int r = 0; r < 4; ++r) {
            y[4 * e + r] = (py[0][r] + py[1][r]) + (py[2][r] + py[3][r]);
            s[4 * e + r] = (ps[0][r] + ps[1][r]) + (ps[2][r] + ps[3][r]);
        }
    }
    float o[8];
#pragma unroll
    for (int j = 0; j < 8; ++j) {
        const float yb = bf_round(y[j]);
        const float gg = bf_round(yb * __builtin_amdgcn_rcpf(1.0f + __expf(-yb)));
        const float sb = bf_round(s[j] * (2.0f / 512.0f));
        const float xx = (j & 1) ? bf_hi(xv[j >> 1]) : bf_lo(xv[j >> 1]);
        o[j] = xx + bf_round(sb * gg);
    }
    const u32x4 ov = {pk2(o[0], o[1]), pk2(o[2], o[3]), pk2(o[4], o[5]), pk2(o[6], o[7])};
    __builtin_amdgcn_raw_buffer_store_b128(ov, rsrc(out, 2097152), out_off(tile64, d0, w, lane), 0, 17);
    STAMP(15);
}

extern "C" void kernel_launch(void* const* d_in, const int* in_sizes, int n_in, void* d_out, int out_size, void* d_ws,
                              size_t ws_size, hipStream_t stream) {
    (void)in_sizes; (void)n_in; (void)out_size; (void)ws_size;
    (void)hipFuncSetAttribute((const void*)fused_kernel, hipFuncAttributeMaxDynamicSharedMemorySize, L_END);
    fused_kernel<<<256, 512, L_END, stream>>>((const char*)d_in[0], (const unsigned short*)d_in[1], (const unsigned short*)d_in[2],
                                              (const char*)d_in[4], (const char*)d_in[5], (char*)d_ws, (char*)d_out);
}
```

```cpp
#include <hip/hip_runtime.h>
#include <stdint.h>
#pragma clang diagnostic ignored "-Wint-to-pointer-cast"

typedef short s16x4 __attribute__((ext_vector_type(4)));
typedef __bf16 bf16x8 __attribute__((ext_vector_type(8)));
typedef __bf16 bf16x2 __attribute__((ext_vector_type(2)));
typedef float f32x4 __attribute__((ext_vector_type(4)));
typedef float f32x2 __attribute__((ext_vector_type(2)));
typedef unsigned u32x4 __attribute__((ext_vector_type(4)));
typedef unsigned u32x2 __attribute__((ext_vector_type(2)));
typedef unsigned long long u64;
#define LAS __attribute__((address_space(3)))
#define GAS __attribute__((address_space(1)))
#define DEVI __device__ __forceinline__

constexpr size_t WS_H = 0;
constexpr size_t WS_X1T = 8u << 20;
constexpr size_t WS_WT = 12u << 20;
constexpr size_t WS_ZT = 16u << 20;
constexpr size_t WS_FLAG = 20u << 20;
constexpr size_t WS_TS = 24u << 20;
constexpr unsigned FL_H = 4096, FL_A = 8192, FL_Z = 8192 + 16384;

constexpr unsigned L_W1B = 65536, L_F0 = 131072, L_F1 = 32768;
constexpr unsigned L_CS = 161792, L_ST = L_CS + 512, L_SY = L_ST + 1024, L_END = L_SY + 64;
constexpr unsigned L_Z = 81920, L_BP = 131072, L_ZS = L_BP + 18432, L_WS = 147456;
constexpr unsigned SY_TEAM = L_SY, SY_ZARR = L_SY + 4, SY_ZOK = L_SY + 8, SY_AARR = L_SY + 12, SY_AOK = L_SY + 16, SY_HARR = L_SY + 20,
                   SY_BOK = L_SY + 24, SY_CS2 = L_SY + 28, SY_H47 = L_SY + 32, SY_W2OK = L_SY + 36, SY_Y6 = L_SY + 40, SY_XR = L_SY + 44;
constexpr unsigned SPIN_CAP = 1u << 16;
constexpr unsigned CODE_PIECES = 11;

#define STAMPC(id) do { } while (0)
#define STAMP4(a, b) do { } while (0)
#define STAMP2(id) do { } while (0)
#define STAMP3(id) do { } while (0)
#define STAMP(id) do { } while (0)

DEVI unsigned pk2(float a, float b) {
    f32x2 f = {a, b};
    return __builtin_bit_cast(unsigned, __builtin_convertvector(f, bf16x2));
}
DEVI float bf_round(float a) { return __builtin_bit_cast(float, pk2(a, a) << 16); }
DEVI float bf_lo(unsigned u) { return __builtin_bit_cast(float, u << 16); }
DEVI float bf_hi(unsigned u) { return __builtin_bit_cast(float, u & 0xffff0000u); }
DEVI f32x4 mfma(u32x4 a, u32x4 b, f32x4 c) {
    return __builtin_amdgcn_mfma_f32_16x16x32_bf16(__builtin_bit_cast(bf16x8, a), __builtin_bit_cast(bf16x8, b), c, 0, 0, 0);
}
DEVI void mfma_ip(f32x4& c, u32x4 a, u32x4 b, bool first) {
    if (first) asm volatile("v_mfma_f32_16x16x32_bf16 %0, %1, %2, 0" : "=v"(c) : "v"(a), "v"(b));
    else asm volatile("v_mfma_f32_16x16x32_bf16 %0, %1, %2, %0" : "+v"(c) : "v"(a), "v"(b));
}
#define MFMA_DONE() asm volatile("s_nop 7\n\ts_nop 1" ::: "memory")
#define WAIT_LGKM(n) __builtin_amdgcn_s_waitcnt(0xC07F | ((n) << 8))
DEVI u32x2 trread(unsigned addr) {
    s16x4 v = __builtin_amdgcn_ds_read_tr16_b64_v4i16((LAS s16x4*)addr);
    return __builtin_bit_cast(u32x2, v);
}
DEVI u32x4 trread2(unsigned a0, unsigned a1) {
    u32x2 lo = trread(a0), hi = trread(a1);
    u32x4 r = {lo[0], lo[1], hi[0], hi[1]};
    return r;
}
DEVI u32x4 lds_rd16(unsigned addr) { return *(LAS u32x4*)addr; }
DEVI void lds_wr16(unsigned addr, u32x4 v) { *(LAS u32x4*)addr = v; }
DEVI void lds_wr8(unsigned addr, u32x2 v) { *(LAS u32x2*)addr = v; }
DEVI float cosr(float rev) { return __builtin_amdgcn_cosf(rev); }
DEVI float sinr(float rev) { return __builtin_amdgcn_sinf(rev); }
DEVI void barrier() { asm volatile("s_barrier" ::: "memory"); }
#define WAIT_VM(n) asm volatile("s_waitcnt vmcnt(%0)" ::"n"(n) : "memory")
#define WAIT_LGKM0() asm volatile("s_waitcnt lgkmcnt(0)" ::: "memory")
#define PIN4(a) asm volatile("" : "+v"(a))
DEVI __amdgpu_buffer_rsrc_t rsrc(char* base, unsigned bytes) { return __builtin_amdgcn_make_buffer_rsrc(base, 0, bytes, 0x00020000); }
DEVI void st_sc1(u32x4 v, __amdgpu_buffer_rsrc_t r, unsigned off) { __builtin_amdgcn_raw_buffer_store_b128(v, r, off, 0, 16); }
DEVI u32x4 ld_sc1(__amdgpu_buffer_rsrc_t r, unsigned off) { return __builtin_amdgcn_raw_buffer_load_b128(r, off, 0, 16); }

DEVI void dma16(unsigned voff, const void* sbase, unsigned dst) {
    unsigned keep;
    asm volatile("s_mov_b32 %0, m0\n\ts_mov_b32 m0, %3\n\ts_nop 0\n\tglobal_load_lds_dwordx4 %1, %2\n\ts_mov_b32 m0, %0"
                 : "=&s"(keep) : "v"(voff), "s"(sbase), "s"(dst) : "memory");
}
template <unsigned IMM>
DEVI void dmaq(unsigned voff, const void* sbase, unsigned sdst) {
    asm volatile("s_add_u32 m0, %2, %3\n\ts_nop 0\n\tglobal_load_lds_dwordx4 %0, %1" :: "v"(voff), "s"(sbase), "s"(sdst), "n"(IMM) : "memory", "scc");
}
DEVI unsigned lds_ld_relaxed(unsigned addr) { return __hip_atomic_load((LAS unsigned*)addr, __ATOMIC_RELAXED, __HIP_MEMORY_SCOPE_WORKGROUP); }
DEVI unsigned lds_arrive(unsigned cnt, int lane) {
    unsigned old = 0;
    if (lane == 0) old = __hip_atomic_fetch_add((LAS unsigned*)cnt, 1u, __ATOMIC_RELAXED, __HIP_MEMORY_SCOPE_WORKGROUP);
    return __builtin_amdgcn_readfirstlane(old);
}
DEVI void lds_wait_ge(unsigned cnt, unsigned target) {
#pragma unroll 1
    for (unsigned s = 0; s < SPIN_CAP * 16; ++s) {
        asm volatile("" ::: "memory");
        if ((unsigned)__builtin_amdgcn_readfirstlane(lds_ld_relaxed(cnt)) >= target) break;
        __builtin_amdgcn_s_sleep(1);
    }
    asm volatile("" ::: "memory");
}
DEVI void team_sync(unsigned cnt, unsigned target, int lane) {
    WAIT_LGKM0();
    if (lane == 0) __hip_atomic_fetch_add((LAS unsigned*)cnt, 1u, __ATOMIC_RELAXED, __HIP_MEMORY_SCOPE_WORKGROUP);
    lds_wait_ge(cnt, target);
}
DEVI u64 epoch_request(const char* ws) { return __hip_atomic_load((const GAS u64*)(ws + WS_FLAG), __ATOMIC_RELAXED, __HIP_MEMORY_SCOPE_AGENT); }
DEVI u64 epoch_finish(u64 e) {
    const u64 v = ((u64)(unsigned)__builtin_amdgcn_readfirstlane((unsigned)(e >> 32)) << 32) | (unsigned)__builtin_amdgcn_readfirstlane((unsigned)e);
    return v + 1;
}
DEVI void post_replicas(char* ws, unsigned off, int b, int lane, u64 epoch) {
    if (lane < 8) __hip_atomic_store((GAS u64*)(ws + WS_FLAG + off + lane * 2048 + b * 8), epoch, __ATOMIC_RELAXED, __HIP_MEMORY_SCOPE_AGENT);
}
template <bool W2>
DEVI void poll256(const char* ws, unsigned off, int b, int lane, u64 epoch) {
    const char* fb = ws + WS_FLAG + off + (b & 7) * 2048;
    const unsigned foff = lane * 16, lo = (unsigned)epoch, hi = (unsigned)(epoch >> 32);
#pragma unroll 1
    for (unsigned s = 0; s < SPIN_CAP; ++s) {
        u32x4 f0, f1;
        asm volatile("global_load_dwordx4 %0, %2, %3 sc1\n\tglobal_load_dwordx4 %1, %2, %3 offset:1024 sc1\n\ts_waitcnt vmcnt(0)"
                     : "=&v"(f0), "=&v"(f1) : "v"(foff), "s"(fb) : "memory");
        const unsigned d = ((f0[0] ^ lo) | (f0[2] ^ lo) | (f1[0] ^ lo) | (f1[2] ^ lo)) | ((f0[1] ^ hi) | (f0[3] ^ hi) | (f1[1] ^ hi) | (f1[3] ^ hi));
        if (W2 && s == 0) (void)lds_arrive(SY_W2OK, lane);
        if (__all(d == 0u)) break;
        __builtin_amdgcn_s_sleep(2);
    }
    asm volatile("" ::: "memory");
}

DEVI unsigned out_off(int tile64, int d0, int w, int lane) {
    asm volatile("" : "+v"(lane));
    const int n = lane & 15, g = lane >> 4, T = w & 3, dp = w >> 2;
    const unsigned tok = 32u * (32 * (T >> 1) + 8 * (n >> 2) + 4 * (T & 1) + (n & 3)) + tile64;
    return tok * 1024u + (d0 + 32 * dp + 8 * g) * 2;
}
DEVI void h_epilogue(const f32x4& a0, const f32x4& a1, unsigned csa, float mb, float rr, __amdgpu_buffer_rsrc_t rh, unsigned off) {
    const f32x4 c0 = *(LAS f32x4*)(csa), c1 = *(LAS f32x4*)(csa + 64);
    float v[8];
#pragma unroll
    for (int r = 0; r < 4; ++r) {
        v[r] = fmaxf(rr * (a0[r] - mb * c0[r]), 0.0f);
        v[4 + r] = fmaxf(rr * (a1[r] - mb * c1[r]), 0.0f);
    }
    const u32x4 o = {pk2(v[0], v[1]), pk2(v[2], v[3]), pk2(v[4], v[5]), pk2(v[6], v[7])};
    st_sc1(o, rh, off);
}

template <int S> struct IC { static constexpr int v = S; };
constexpr int H_RING = 6;
DEVI unsigned y_blk(int i, unsigned yrot) { const unsigned t = yrot + i; return (t >= 14) ? t - 14 : t; }
constexpr int y_slot(int i) { return i < 4 ? i : i < 7 ? i + 1 : i < 12 ? i - 7 : i - 12; }
template <int I>
DEVI void w2_step(const char* wb, unsigned voffw, unsigned sd, unsigned yrot) {
    const char* sb = wb + (size_t)(y_blk(I, yrot) * (128u * 1024u));
    dmaq<y_slot(I) * 16384>(voffw, sb, sd);
    dmaq<y_slot(I) * 16384 + 1024>(voffw + 8192, sb, sd);
    dmaq<y_slot(I) * 16384 + 2048>(voffw + 16384, sb, sd);
    dmaq<y_slot(I) * 16384 + 3072>(voffw + 24576, sb, sd);
}
DEVI void f_gen(int fg, unsigned foff, int rot, int u, int lane) {
    const int n = lane & 15, g = lane >> 4;
    const int c = 16 * fg + n;
    const int k2 = (c == 1) ? 256 : (c >> 1), part = (c == 1) ? 0 : (c & 1);
    const float st = (float)k2 * (1.0f / 512.0f), ph = (float)part * 0.25f;
    const unsigned sg = (k2 & 1) ? 0x80008000u : 0u;
    const float d0 = (float)(64 * u + 8 * g + 64 * rot);
#pragma unroll
    for (int e = 0; e < 2; ++e) {
        const float x0 = __builtin_amdgcn_fractf(st * (d0 + 32.0f * e) + ph);
        float v[8];
#pragma unroll
        for (int j = 0; j < 8; ++j) v[j] = cosr(x0 + (float)j * st);
        const u32x4 o = {pk2(v[0], v[1]), pk2(v[2], v[3]), pk2(v[4], v[5]), pk2(v[6], v[7])};
        const u32x4 o2 = {o[0] ^ sg, o[1] ^ sg, o[2] ^ sg, o[3] ^ sg};
        lds_wr16(foff + (2 * u + e) * 1024 + lane * 16, o);
        lds_wr16(foff + (2 * u + e + 8) * 1024 + lane * 16, o2);
    }
}
template <int NF, int NG>
DEVI void m_first(const char* x, char* ws, u32x4 (&xr)[16][2], float (&mb)[2], float (&rr)[2], __amdgpu_buffer_rsrc_t rh, unsigned hoff,
                  int P, int w, int lane, int fg0, int rot) {
    const int n = lane & 15, g = lane >> 4, q = n >> 2, p = n & 3;
    const int t0 = 2 * P + (w >> 1);
    unsigned vx[4];
    {
        const unsigned r8 = lane >> 3, c8 = lane & 7;
        const unsigned v0 = (32u * (32 * (w & 1) + 8 * (r8 >> 2) + (r8 & 3)) + t0) * 1024u + ((c8 ^ (r8 >> 1)) * 16);
        vx[0] = v0; vx[1] = (v0 ^ 64u) + 524288u; vx[2] = v0 + 131072u; vx[3] = (v0 ^ 64u) + 524288u + 131072u;
    }
    const unsigned sdx = L_W1B + w * 4096;
    auto dmx = [&](auto J) {
        constexpr int j = decltype(J)::v;
        const char* sb = x + (((j + rot) & 7) << 7);
        dmaq<(j & 3) * 16384>(vx[0], sb, sdx); dmaq<(j & 3) * 16384 + 1024>(vx[1], sb, sdx);
        dmaq<(j & 3) * 16384 + 2048>(vx[2], sb, sdx); dmaq<(j & 3) * 16384 + 3072>(vx[3], sb, sdx);
    };
    const unsigned xa0 = sdx + n * 128 + ((g ^ ((n >> 1) & 7)) * 16), xa1 = xa0 ^ 64u;
    auto rdx = [&](int j) {
        xr[2 * j][0] = lds_rd16(xa0 + (j & 3) * 16384); xr[2 * j][1] = lds_rd16(xa0 + (j & 3) * 16384 + 2048);
        xr[2 * j + 1][0] = lds_rd16(xa1 + (j & 3) * 16384); xr[2 * j + 1][1] = lds_rd16(xa1 + (j & 3) * 16384 + 2048);
    };
    dmx(IC<0>{}); dmx(IC<1>{});
    if (NF == 3) { f_gen(fg0 + 1, L_F1, rot, w, lane); f_gen(fg0 + 2, L_F1 + 16384, rot, w, lane); }
    else f_gen(fg0, L_F0, rot, w, lane);
    dmx(IC<2>{}); dmx(IC<3>{});
    STAMP(1);
    WAIT_VM(12); rdx(0); WAIT_LGKM0();
    f32x4 acc[2][NG], af[2][NF], aS[2], aQ[2];
    u32x4 ones = {0x3f803f80u, 0x3f803f80u, 0x3f803f80u, 0x3f803f80u};
    PIN4(ones);
    {
        constexpr unsigned PITCH = (NG == 4) ? 128 : 64;
        const unsigned swz = (NG == 4) ? ((q >> 1) | ((g & 1) << 1)) : (g & 1);
        const unsigned abase = (8 * g + q) * PITCH + swz * 32 + p * 8;
        u32x4 A[NG], F[NF];
        auto rdA = [&](int ks, int db) -> u32x4 { const unsigned a = (abase ^ (db << 5)) + ks * 32 * PITCH; return trread2(a, a + 4 * PITCH); };
        auto rdF = [&](int ks, int f) -> u32x4 { return lds_rd16(((f == 0) ? L_F0 : L_F1 + (f - 1) * 16384) + ks * 1024 + lane * 16); };
        __builtin_amdgcn_s_setprio(2);
        barrier();
        STAMP(2);
#pragma unroll
        for (int db = 0; db < NG; ++db) A[db] = rdA(0, db);
#pragma unroll
        for (int f = 0; f < NF; ++f) F[f] = rdF(0, f);
#pragma unroll
        for (int ks = 0; ks < 16; ++ks) {
            STAMP2(ks);
            if (ks == 0) STAMP4(84, 85); if (ks == 2) STAMP4(86, 87); if (ks == 4) STAMP4(88, 89); if (ks == 6) STAMP4(90, 91);
            if (ks == 8) STAMP4(92, 93); if (ks == 10) STAMP4(94, 95); if (ks == 12) STAMP4(96, 97); if (ks == 14) STAMP4(98, 99);
            if (ks == 1 || ks == 3 || ks == 7) barrier();
            if (ks == 0) dmx(IC<4>{});
            if (ks == 2) { WAIT_LGKM(7); dmx(IC<5>{}); }
            if (ks == 4) { WAIT_LGKM(7); dmx(IC<6>{}); }
            if (ks == 6) { WAIT_LGKM(7); dmx(IC<7>{}); }
            if (ks == 1 || ks == 3 || ks == 5 || ks == 7) { WAIT_VM(12); rdx((ks + 1) / 2); }
            if (ks == 9) { WAIT_VM(8); rdx(5); }
            if (ks == 11) { WAIT_VM(4); rdx(6); }
            if (ks == 13) { WAIT_VM(0); rdx(7); }
            if ((ks == 7 || ks == 9 || ks == 11 || ks == 13) && lane == 0)
                (void)__hip_atomic_fetch_add((LAS unsigned*)SY_XR, 1u, __ATOMIC_RELAXED, __HIP_MEMORY_SCOPE_WORKGROUP);
#pragma unroll
            for (int db = 0; db < 4; ++db) {
                if (db < NG) {
                    mfma_ip(acc[0][db], A[db], xr[ks][0], ks == 0);
                    mfma_ip(acc[1][db], A[db], xr[ks][1], ks == 0);
                    if (ks < 15) A[db] = rdA(ks + 1, db);
                }
                if (db < 2) mfma_ip(aS[db], ones, xr[ks][db], ks == 0);
                else mfma_ip(aQ[db - 2], xr[ks][db - 2], xr[ks][db - 2], ks == 0);
                __builtin_amdgcn_sched_barrier(0);
            }
#pragma unroll
            for (int f = 0; f < NF; ++f) {
                mfma_ip(af[0][f], xr[ks][0], F[f], ks == 0);
                mfma_ip(af[1][f], xr[ks][1], F[f], ks == 0);
                if (ks < 15) F[f] = rdF(ks + 1, f);
                __builtin_amdgcn_sched_barrier(0);
            }
        }
    }
    MFMA_DONE();
#pragma unroll
    for (int tt = 0; tt < 2; ++tt) {
        PIN4(aS[tt]); PIN4(aQ[tt]);
#pragma unroll
        for (int i = 0; i < NG; ++i) PIN4(acc[tt][i]);
#pragma unroll
        for (int f = 0; f < NF; ++f) PIN4(af[tt][f]);
    }
    STAMP(3);
#pragma unroll
    for (int tt = 0; tt < 2; ++tt) {
        const float sq = (p & 2) ? ((p & 1) ? aQ[tt][3] : aQ[tt][2]) : ((p & 1) ? aQ[tt][1] : aQ[tt][0]);
        const float mean = aS[tt][0] * (1.0f / 512.0f);
        const float var = fmaxf(sq * (1.0f / 512.0f) - mean * mean, 0.0f);
        const float m = bf_round(mean);
        const float den = bf_round(__builtin_sqrtf(bf_round(bf_round(var) + 1e-5f)));
        const float r = 1.0f / den;
        if (g == q) lds_wr8(L_ST + (w * 32 + tt * 16 + n) * 8, u32x2{__builtin_bit_cast(unsigned, m), __builtin_bit_cast(unsigned, r)});
    }
    WAIT_LGKM0();
    {
        f32x4 st[2][2];
#pragma unroll
        for (int tt = 0; tt < 2; ++tt) {
            st[tt][0] = *(LAS f32x4*)(L_ST + (w * 32 + tt * 16 + 4 * g) * 8);
            st[tt][1] = *(LAS f32x4*)(L_ST + (w * 32 + tt * 16 + 4 * g) * 8 + 16);
            const f32x2 s = *(LAS f32x2*)(L_ST + (w * 32 + tt * 16 + n) * 8);
            mb[tt] = s[0]; rr[tt] = s[1];
        }
        const __amdgpu_buffer_rsrc_t rx = rsrc(ws + WS_X1T, 512 * 4096);
#pragma unroll
        for (int f = 0; f < NF; ++f) {
            const int c = 16 * (fg0 + f) + n;
            const float cs = (c == 0) ? 512.0f : 0.0f;
            float v[8];
#pragma unroll
            for (int tt = 0; tt < 2; ++tt)
#pragma unroll
                for (int r = 0; r < 4; ++r) v[4 * tt + r] = st[tt][r >> 1][2 * (r & 1) + 1] * (af[tt][f][r] - st[tt][r >> 1][2 * (r & 1)] * cs);
            const u32x4 o = {pk2(v[0], v[1]), pk2(v[2], v[3]), pk2(v[4], v[5]), pk2(v[6], v[7])};
            st_sc1(o, rx, (c >> 1) * 8192 + (4 * P + w) * 128 + (c & 1) * 64 + g * 16);
        }
    }
    STAMP(5);
    __builtin_amdgcn_s_setprio(0);
    barrier();
    STAMP(4);
#pragma unroll
    for (int i2 = 0; i2 < NG / 2; ++i2)
#pragma unroll
        for (int tt = 0; tt < 2; ++tt)
            h_epilogue(acc[tt][2 * i2], acc[tt][2 * i2 + 1], L_CS + (32 * i2 + 4 * g) * 4, mb[tt], rr[tt], rh, hoff + (i2 * 4 + tt) * 1024);
}

DEVI void m_phase_a(const char* x, char* ws, const char* wb, unsigned voffw, int b, int P, bool typeA, int w, int lane, int ii0, int fg0, int hj, int rot, u64& epoch) {
    const int n = lane & 15, g = lane >> 4, q = n >> 2, p = n & 3;
    const u64 ereq = epoch_request(ws);
    u32x4 xr[16][2];
    float mb[2], rr[2];
    const __amdgpu_buffer_rsrc_t rh = rsrc(ws + WS_H, 8u << 20);
    const unsigned hoff = (((2 * P + (w >> 1)) * 56 + ii0) * 4 + 2 * (w & 1)) * 1024 + lane * 16;
    if (typeA) m_first<1, 4>(x, ws, xr, mb, rr, rh, hoff, P, w, lane, fg0, rot);
    else m_first<3, 2>(x, ws, xr, mb, rr, rh, hoff, P, w, lane, fg0, rot);
    if (typeA) WAIT_VM(4); else WAIT_VM(2);
    epoch = epoch_finish(ereq);
    if (lds_arrive(SY_AARR, lane) == 7) post_replicas(ws, FL_A, b, lane, epoch);
    lds_wait_ge(SY_BOK, 4);
    STAMP(6); STAMPC(12);
    f32x4 a2[2][4];
    {
        const unsigned swz = (q >> 1) | ((g & 1) << 1);
        const unsigned abase = L_W1B + (8 * g + q) * 128 + swz * 32 + p * 8;
        u32x2 Al[8], Ah[8];
        auto adr = [&](int j) -> unsigned { return (abase ^ ((j & 3) << 5)) + (j >> 2) * 4096; };
#pragma unroll
        for (int j = 0; j < 7; ++j) { Al[j] = trread(adr(j)); Ah[j] = trread(adr(j) + 512); }
        __builtin_amdgcn_sched_barrier(0);
#pragma unroll
        for (int j = 0; j < 64; ++j) {
            if ((j & 3) == 0) { if (j + 7 < 64) WAIT_LGKM(6); else WAIT_LGKM(0); }
            const u32x4 a = {Al[j & 7][0], Al[j & 7][1], Ah[j & 7][0], Ah[j & 7][1]};
            mfma_ip(a2[0][j & 3], a, xr[j >> 2][0], j < 4);
            __builtin_amdgcn_sched_barrier(0);
            if (j + 7 < 64) Al[(j + 7) & 7] = trread(adr(j + 7));
            __builtin_amdgcn_sched_barrier(0);
            mfma_ip(a2[1][j & 3], a, xr[j >> 2][1], j < 4);
            __builtin_amdgcn_sched_barrier(0);
            if (j + 7 < 64) Ah[(j + 7) & 7] = trread(adr(j + 7) + 512);
            __builtin_amdgcn_sched_barrier(0);
        }
        MFMA_DONE();
#pragma unroll
        for (int db = 0; db < 4; ++db) { PIN4(a2[0][db]); PIN4(a2[1][db]); }
    }
    WAIT_LGKM0();
    (void)lds_arrive(SY_H47, lane);
    STAMP(7); STAMPC(13);
    lds_wait_ge(SY_CS2, 4);
    const unsigned hoff2 = hoff + (typeA ? 8192 : 4096);
#pragma unroll
    for (int i2 = 0; i2 < 2; ++i2)
#pragma unroll
        for (int tt = 0; tt < 2; ++tt)
            h_epilogue(a2[tt][2 * i2], a2[tt][2 * i2 + 1], L_CS + (64 + 32 * i2 + 4 * g) * 4, mb[tt], rr[tt], rh, hoff2 + (i2 * 4 + tt) * 1024);
    WAIT_VM(0);
    if (lds_arrive(SY_HARR, lane) == 3 && lane == 0)
        __hip_atomic_store((GAS u64*)(ws + WS_FLAG + FL_H + P * 128 + hj * 8), epoch, __ATOMIC_RELAXED, __HIP_MEMORY_SCOPE_AGENT);
    STAMP(8);
}

#define GLOAD16(dst, voff, sbase, imm) asm volatile("global_load_dwordx4 %0, %1, %2 offset:%3 sc1" : "=v"(dst) : "v"(voff), "s"(sbase), "n"(imm) : "memory")
constexpr int y_ops(int j) { return ((j == 4 || j == 5) ? 4 : 0) + ((j + H_RING < 14) ? 4 : 0); }
constexpr int y_after(int i) {
    int cnt = (i < H_RING) ? 4 * (H_RING - 1 - i) : 0;
    for (int j = (i < H_RING) ? 0 : (i - (H_RING - 1)); j < i; ++j) cnt += y_ops(j);
    if (i == 8 || i == 9) {
        int c2 = (i - 4 + H_RING < 14) ? 4 : 0;
        for (int j = i - 3; j < i; ++j) c2 += y_ops(j);
        if (c2 < cnt) cnt = c2;
    }
    return cnt;
}

DEVI void m_team(const char* x, const char* __restrict__ w2, char* ws, int b, int P, bool typeA, int ii0, int fg0, int hj,
                 int tile64, int d0, int rot, unsigned yrot, int w, int lane, u32x4& xv) {
    const int n = lane & 15, g = lane >> 4, q = n >> 2, p = n & 3;
    const int r8 = lane >> 3, p16 = lane & 7;
    const unsigned voffw = r8 * 1024 + ((p16 ^ (2 * ((r8 >> 1) & 3))) * 16);
    const char* wb = w2 + d0 * 2 + (size_t)w * 32 * 1024;
    u64 epoch;
    m_phase_a(x, ws, wb, voffw, b, P, typeA, w, lane, ii0, fg0, hj, rot, epoch);

    const unsigned sdw = w * 4096;
    const unsigned abase = w * 4096 + (4 * g + q) * 128 + (((2 * g + (q >> 1)) & 3) * 32) + p * 8;
    u32x4 A[4][4];
    auto rdA = [&](int i) {
#pragma unroll
        for (int db = 0; db < 4; ++db) { const unsigned a = (abase ^ (db << 5)) + y_slot(i) * 16384; A[i & 3][db] = trread2(a, a + 2048); }
    };
    lds_wait_ge(SY_W2OK, 4);
    rdA(0); rdA(1); rdA(2); rdA(3);
    lds_wait_ge(SY_H47, 4);
    w2_step<4>(wb, voffw, sdw, yrot); w2_step<5>(wb, voffw, sdw, yrot); w2_step<6>(wb, voffw, sdw, yrot); w2_step<11>(wb, voffw, sdw, yrot);
#pragma unroll
    for (int i = 0; i < 4; ++i)
#pragma unroll
        for (int db = 0; db < 4; ++db) PIN4(A[i][db]);
    WAIT_LGKM0();
    w2_step<7>(wb, voffw, sdw, yrot); w2_step<8>(wb, voffw, sdw, yrot); w2_step<9>(wb, voffw, sdw, yrot); w2_step<10>(wb, voffw, sdw, yrot);
    {
        const char* fb = ws + WS_FLAG + FL_H + (tile64 >> 1) * 128;
        const unsigned foff = (lane & 15) * 8, lo = (unsigned)epoch, hi = (unsigned)(epoch >> 32);
#pragma unroll 1
        for (unsigned s = 0; s < SPIN_CAP; ++s) {
            u32x2 f;
            asm volatile("global_load_dwordx2 %0, %1, %2 sc1\n\ts_waitcnt vmcnt(0)" : "=v"(f) : "v"(foff), "s"(fb) : "memory");
            if (__all(((f[0] ^ lo) | (f[1] ^ hi)) == 0u)) break;
            __builtin_amdgcn_s_sleep(2);
        }
        asm volatile("" ::: "memory");
    }
    STAMP(9); STAMP3(15);
    const char* hb = ws + WS_H + (size_t)(tile64 * 56 + w) * 4096;
    const unsigned voffh = lane * 16;
    u32x4 h[H_RING][4];
    auto issue_h = [&](int i) {
        const char* hp = hb + (size_t)(y_blk(i, yrot) * 16384u);
        GLOAD16(h[i % H_RING][0], voffh, hp, 0); GLOAD16(h[i % H_RING][1], voffh, hp, 1024);
        GLOAD16(h[i % H_RING][2], voffh, hp, 2048); GLOAD16(h[i % H_RING][3], voffh, hp, 3072);
    };
#pragma unroll
    for (int i = 0; i < H_RING; ++i) issue_h(i);
    f32x4 ay[4][4];
#define T1_WAIT(N, slot) asm volatile("s_waitcnt vmcnt(%4)" : "+v"(h[slot][0]), "+v"(h[slot][1]), "+v"(h[slot][2]), "+v"(h[slot][3]) : "n"(N) : "memory")
    auto ystep = [&](auto I) {
        constexpr int i = decltype(I)::v;
        T1_WAIT(y_after(i), i % H_RING);
        if (i == 0) STAMP(10);
        STAMP3(i);
#pragma unroll
        for (int db = 0; db < 4; ++db)
#pragma unroll
            for (int t = 0; t < 4; ++t) mfma_ip(ay[db][t], A[i & 3][db], h[i % H_RING][t], i == 0);
        __builtin_amdgcn_sched_barrier(0);
        if (i + 4 < 14) rdA(i + 4);
        if (i == 2) (void)lds_arrive(SY_Y6, lane);
        if constexpr (i == 4 || i == 5) { WAIT_LGKM0(); w2_step<i + 8>(wb, voffw, sdw, yrot); }
        if (i + H_RING < 14) issue_h(i + H_RING);
        if (i == 10) xv = *(const GAS u32x4*)((const GAS char*)x + out_off(tile64, d0, w, lane));
        __builtin_amdgcn_sched_barrier(0);
    };
    ystep(IC<0>{}); ystep(IC<1>{}); ystep(IC<2>{}); ystep(IC<3>{}); ystep(IC<4>{}); ystep(IC<5>{}); ystep(IC<6>{});
    ystep(IC<7>{}); ystep(IC<8>{}); ystep(IC<9>{}); ystep(IC<10>{}); ystep(IC<11>{}); ystep(IC<12>{}); ystep(IC<13>{});
    MFMA_DONE();
#pragma unroll
    for (int t = 0; t < 4; ++t)
#pragma unroll
        for (int db = 0; db < 4; ++db) PIN4(ay[db][t]);
    STAMP(11); STAMP3(14);
#pragma unroll
    for (int t = 0; t < 4; ++t)
#pragma unroll
        for (int db = 0; db < 4; ++db) *(LAS f32x4*)((t * 4 + w) * 4096 + db * 1024 + lane * 16) = ay[db][t];
}

template <bool HAS_I, bool FIRST>
DEVI void p2_unit(char* ws, const u32x4 (&Ur)[2][2], const u32x4 (&Ui)[2][2], const u32x4 (&Wf)[2], int zrow, int v, int lane, unsigned& sync_n,
                  const u32x4 (&C64)[2], const u32x4 (&S64)[2], const u32x4 (&N64)[2],
                  const u32x4 (&C32)[2], const u32x4 (&S32)[2], const u32x4 (&N32)[2],
                  const float (&tc)[8], const float (&tsn)[8]) {
    const int n = lane & 15, g = lane >> 4, q = n >> 2, p = n & 3;
    STAMP3(2);
    float br[8], bi[8];
#pragma unroll
    for (int mt = 0; mt < 2; ++mt) {
        f32x4 dr = {0, 0, 0, 0}, di = {0, 0, 0, 0};
#pragma unroll
        for (int ks = 0; ks < 2; ++ks) {
            dr = mfma(Ur[mt][ks], C64[ks], dr);
            di = mfma(Ur[mt][ks], N64[ks], di);
            if (HAS_I) { dr = mfma(Ui[mt][ks], S64[ks], dr); di = mfma(Ui[mt][ks], C64[ks], di); }
        }
#pragma unroll
        for (int r = 0; r < 4; ++r) {
            const float c = tc[4 * mt + r], s = tsn[4 * mt + r];
            br[4 * mt + r] = dr[r] * c + di[r] * s;
            bi[4 * mt + r] = di[r] * c - dr[r] * s;
        }
    }
    u32x4 Br = {pk2(br[0], br[1]), pk2(br[2], br[3]), pk2(br[4], br[5]), pk2(br[6], br[7])};
    u32x4 Bi = {pk2(bi[0], bi[1]), pk2(bi[2], bi[3]), pk2(bi[4], bi[5]), pk2(bi[6], bi[7])};
    float yr[8], yi[8];
#pragma unroll
    for (int t = 0; t < 2; ++t) {
        f32x4 dr = {0, 0, 0, 0}, di = {0, 0, 0, 0};
        dr = mfma(C32[t], Br, dr); dr = mfma(S32[t], Bi, dr);
        di = mfma(C32[t], Bi, di); di = mfma(N32[t], Br, di);
#pragma unroll
        for (int r = 0; r < 4; ++r) {
            const float wre = bf_lo(Wf[t][r]), wim = bf_hi(Wf[t][r]);
            yr[4 * t + r] = dr[r] * wre - di[r] * wim;
            yi[4 * t + r] = dr[r] * wim + di[r] * wre;
        }
    }
    u32x4 Yr = {pk2(yr[0], yr[1]), pk2(yr[2], yr[3]), pk2(yr[4], yr[5]), pk2(yr[6], yr[7])};
    u32x4 Yi = {pk2(yi[0], yi[1]), pk2(yi[2], yi[3]), pk2(yi[4], yi[5]), pk2(yi[6], yi[7])};
#pragma unroll
    for (int mt = 0; mt < 2; ++mt) {
        f32x4 dr = {0, 0, 0, 0}, di = {0, 0, 0, 0};
        dr = mfma(C32[mt], Yr, dr); dr = mfma(N32[mt], Yi, dr);
        di = mfma(C32[mt], Yi, di); di = mfma(S32[mt], Yr, di);
        float pr[4], pi[4];
#pragma unroll
        for (int r = 0; r < 4; ++r) {
            const float c = tc[4 * mt + r] * (1.0f / 2048.0f), s = tsn[4 * mt + r] * (1.0f / 2048.0f);
            pr[r] = dr[r] * c - di[r] * s;
            pi[r] = di[r] * c + dr[r] * s;
        }
        const unsigned a = L_BP + (16 * v + n) * 72 + (16 * mt + 4 * g) * 2;
        lds_wr8(a, u32x2{pk2(pr[0], pr[1]), pk2(pr[2], pr[3])});
        lds_wr8(a + 64 * 72, u32x2{pk2(pi[0], pi[1]), pk2(pi[2], pi[3])});
    }
    STAMP3(3);
    sync_n += 4; team_sync(SY_TEAM, sync_n, lane);
    STAMP3(4);
#pragma unroll
    for (int nt = 0; nt < 2; ++nt) {
        f32x4 dr = {0, 0, 0, 0}, di = {0, 0, 0, 0};
#pragma unroll
        for (int ks = 0; ks < 2; ++ks) {
            const unsigned a = L_BP + (32 * ks + 8 * g + q) * 72 + nt * 32 + p * 8;
            const u32x4 pr = trread2(a, a + 4 * 72), pi = trread2(a + 64 * 72, a + 64 * 72 + 4 * 72);
            dr = mfma(C64[ks], pr, dr); dr = mfma(N64[ks], pi, dr);
            if (HAS_I) { di = mfma(C64[ks], pi, di); di = mfma(S64[ks], pr, di); }
        }
        const unsigned a = L_ZS + (16 * nt + n) * 144 + (16 * v + 4 * g) * 2;
        lds_wr8(a, u32x2{pk2(dr[0], dr[1]), pk2(dr[2], dr[3])});
        if (HAS_I) lds_wr8(a + 32 * 144, u32x2{pk2(di[0], di[1]), pk2(di[2], di[3])});
    }
    STAMP3(5);
    sync_n += 4; team_sync(SY_TEAM, sync_n, lane);
    STAMP3(6);
    {
        const __amdgpu_buffer_rsrc_t rs = rsrc(ws + WS_ZT, 512 * 4096);
#pragma unroll
        for (int it = 0; it < (HAS_I ? 2 : 1); ++it) {
            const int row = 8 * v + (lane >> 3), ch = lane & 7;
            const u32x4 o = lds_rd16(L_ZS + (it * 32 + row) * 144 + ch * 16);
            st_sc1(o, rs, (zrow + it) * 4096 + row * 128 + ch * 16);
        }
    }
}

#define GLOAD_U16(dst, voff, sbase, imm) asm volatile("global_load_ushort %0, %1, %2 offset:%3" : "=v"(dst) : "v"(voff), "s"(sbase), "n"(imm) : "memory")
DEVI void g_gen(u32x4 (&G)[4], int dbase, int v, int lane) {
    const int n = lane & 15, g = lane >> 4;
    const int d = dbase + n;
    float c[4], s[4];
#pragma unroll
    for (int jj = 0; jj < 4; ++jj) {
        const float a = (float)(((64 * v + 4 * g + jj) * d) & 511) * (1.0f / 512.0f);
        c[jj] = cosr(a); s[jj] = sinr(a);
    }
    const float ar = (float)((16 * d) & 511) * (1.0f / 512.0f);
    const float cr = cosr(ar), sr = sinr(ar);
#pragma unroll
    for (int ks = 0; ks < 4; ++ks) {
        u32x4 o;
#pragma unroll
        for (int jj = 0; jj < 4; ++jj) o[jj] = pk2(c[jj], -s[jj]);
        if (ks == 0 && v == 0 && g == 0) o[0] = pk2(0.5f, (d & 1) ? -0.5f : 0.5f);
        G[ks] = o;
        PIN4(G[ks]);
        if (ks < 3) {
#pragma unroll
            for (int jj = 0; jj < 4; ++jj) {
                const float c2 = c[jj] * cr - s[jj] * sr, s2 = s[jj] * cr + c[jj] * sr;
                c[jj] = c2; s[jj] = s2;
            }
        }
    }
}
DEVI void s_team(const char* x, const char* __restrict__ w1, const char* __restrict__ w2, const unsigned short* wr, const unsigned short* wi, char* ws, int b,
                 bool typeA, int col0, int fg0, int tile64, int d0, int rot, unsigned yrot, int u, int lane, u32x4& xv) {
    const int n = lane & 15, g = lane >> 4, q = n >> 2, p = n & 3;
    const u64 ereq = epoch_request(ws);
    const char* wb = w1 + col0 * 2;
    const int r8 = lane >> 3, p16 = lane & 7;
    const unsigned v128 = r8 * 3584 + (p16 & 1) * 16;
    unsigned va[2];
    if (typeA) {
#pragma unroll
        for (int j = 0; j < 2; ++j) va[j] = v128 + (((p16 >> 1) ^ (((r8 >> 1) & 1) | (j << 1))) * 32) + (2 * u + j) * 8 * 3584;
    } else {
        const int r16 = lane >> 2, c4 = lane & 3;
        va[0] = va[1] = r16 * 3584 + (((c4 >> 1) ^ (r16 >> 3)) * 32) + (c4 & 1) * 16 + u * 16 * 3584;
    }
    const unsigned sdA = typeA ? u * 2048 : u * 1024;
    auto stage_a = [&](auto S) {
        constexpr int s = decltype(S)::v;
        const char* sb = wb + (size_t)(((s + rot) & 7) * (64 * 3584));
        if (typeA) { dmaq<s * 8192>(va[0], sb, sdA); dmaq<s * 8192 + 1024>(va[1], sb, sdA); }
        else dmaq<s * 4096>(va[0], sb, sdA);
    };
    const int rs = b & 63, cq = b >> 6;
    const int jv = rs & 3, ja = (rs >> 2) & 1, jt = (rs >> 3) & 1, jg = rs >> 4;
    unsigned wv[2][4][2], wsp;
    auto filter_loads = [&]() {
        const unsigned k0 = 16 * jv + 8 * ja + 2 * u + 64 * (16 * jt + 4 * jg);
        const unsigned off0 = (k0 * 257 + 64 * cq + lane) * 2;
#pragma unroll
        for (int r = 0; r < 4; ++r) {
            const unsigned off = off0 + r * (64 * 514);
            GLOAD_U16(wv[0][r][0], off, wr, 0); GLOAD_U16(wv[0][r][1], off, wi, 0);
            GLOAD_U16(wv[1][r][0], off, wr, 514); GLOAD_U16(wv[1][r][1], off, wi, 514);
        }
        const int unit = 2 * b + ((lane >> 3) & 1), reg = (lane >> 1) & 3;
        const unsigned k1 = 16 * (unit >> 7) + (unit & 15) + 64 * (16 * ((unit >> 6) & 1) + 4 * ((unit >> 4) & 3) + reg);
        const unsigned long long a = (unsigned long long)((lane & 1) ? wi : wr) + (k1 * 257 + 256) * 2;
        asm volatile("global_load_ushort %0, %1, off" : "=v"(wsp) : "v"(a) : "memory");
    };
    const char* wb2 = wb + (typeA ? 128 : 64);
    const unsigned vbb = v128 + (((p16 >> 1) ^ (((r8 >> 1) & 1) | ((u & 1) << 1))) * 32) + u * 8 * 3584;
    const unsigned sdB = L_W1B + u * 1024;
    auto stage_b = [&](auto K) {
        constexpr int k = decltype(K)::v;
        dmaq<k * 4096>(vbb, wb2 + (size_t)(((k + 2 * rot) & 15) * (32 * 3584)), sdB);
    };
    u32x4 C64[2], S64[2], N64[2], C32[2], S32[2], N32[2];
    float tc[8], tsn[8];
    auto gen_c64 = [&]() {
        const int m = 16 * u + n;
        const float st = (float)m * (1.0f / 64.0f), a0 = __builtin_amdgcn_fractf(st * (float)(8 * g));
        float c[8], sn[8];
#pragma unroll
        for (int j = 0; j < 8; ++j) { const float a = a0 + (float)j * st; c[j] = cosr(a); sn[j] = sinr(a); }
        const unsigned sg = (m & 1) ? 0x80008000u : 0u;
#pragma unroll
        for (int e = 0; e < 4; ++e) {
            C64[0][e] = pk2(c[2 * e], c[2 * e + 1]); S64[0][e] = pk2(sn[2 * e], sn[2 * e + 1]); N64[0][e] = S64[0][e] ^ 0x80008000u;
            C64[1][e] = C64[0][e] ^ sg; S64[1][e] = S64[0][e] ^ sg; N64[1][e] = N64[0][e] ^ sg;
        }
#pragma unroll
        for (int e = 0; e < 2; ++e) { PIN4(C64[e]); PIN4(S64[e]); PIN4(N64[e]); }
    };
    auto gen_c32 = [&]() {
        const float st = (float)n * (1.0f / 32.0f), a0 = st * (float)(4 * g);
        float c[4], sn[4];
#pragma unroll
        for (int j = 0; j < 4; ++j) { const float a = a0 + (float)j * st; c[j] = cosr(a); sn[j] = sinr(a); }
        const unsigned sg = (n & 1) ? 0x80008000u : 0u;
#pragma unroll
        for (int e = 0; e < 2; ++e) {
            C32[0][e] = pk2(c[2 * e], c[2 * e + 1]); S32[0][e] = pk2(sn[2 * e], sn[2 * e + 1]);
            C32[0][e + 2] = C32[0][e] ^ sg; S32[0][e + 2] = S32[0][e] ^ sg;
        }
#pragma unroll
        for (int e = 0; e < 4; ++e) {
            C32[1][e] = C32[0][e] ^ 0x80000000u; S32[1][e] = S32[0][e] ^ 0x80000000u;
            N32[0][e] = S32[0][e] ^ 0x80008000u; N32[1][e] = S32[0][e] ^ 0x00008000u;
        }
#pragma unroll
        for (int e = 0; e < 2; ++e) { PIN4(C32[e]); PIN4(S32[e]); PIN4(N32[e]); }
    };
    auto gen_tw = [&]() {
        const float st = (float)(16 * u + n) * (1.0f / 2048.0f), a0 = st * (float)(4 * g);
#pragma unroll
        for (int j = 0; j < 8; ++j) { const float a = a0 + (float)(16 * (j >> 2) + (j & 3)) * st; tc[j] = cosr(a); tsn[j] = sinr(a); }
#pragma unroll
        for (int j = 0; j < 8; ++j) { PIN4(tc[j]); PIN4(tsn[j]); }
    };
    const u32x4 ones = {0x3f803f80u, 0x3f803f80u, 0x3f803f80u, 0x3f803f80u};
    {
        const unsigned swz = (q >> 1) | ((g & 1) << 1);
        const unsigned abase = typeA ? (((8 * g + q) * 128 + swz * 32 + p * 8) ^ (u << 5)) : ((8 * g + q) * 64 + (((u ^ g) & 1) * 32) + p * 8);
        const unsigned kpitch = typeA ? 4096 : 2048, hpitch = typeA ? 512 : 256;
        f32x4 cs = {0, 0, 0, 0};
        auto col_sums = [&](int s) {
            const unsigned a0 = abase + (2 * s) * kpitch, a1 = a0 + kpitch;
            const u32x4 c0 = trread2(a0, a0 + hpitch), c1 = trread2(a1, a1 + hpitch);
            cs = mfma(c0, ones, cs);
            cs = mfma(c1, ones, cs);
        };
        stage_a(IC<0>{}); stage_a(IC<1>{});
        if (!typeA) f_gen(fg0, L_F0, rot, u, lane);
        stage_a(IC<2>{}); stage_a(IC<3>{});
        STAMP(1);
        if (typeA) WAIT_VM(6); else WAIT_VM(3);
        WAIT_LGKM0();
        STAMP(2);
        barrier();
        STAMP(3);
        filter_loads();
        stage_a(IC<4>{}); stage_a(IC<5>{});
        col_sums(0);
        if (typeA) WAIT_VM(25); else WAIT_VM(21);
        barrier();
        stage_a(IC<6>{}); stage_a(IC<7>{});
        col_sums(1);
        if (typeA) WAIT_VM(25); else WAIT_VM(21);
        barrier();
        col_sums(2); col_sums(3);
        WAIT_VM(0);
        asm volatile("" : "+v"(wv[0][0][0]), "+v"(wv[0][0][1]), "+v"(wv[0][1][0]), "+v"(wv[0][1][1]), "+v"(wv[0][2][0]), "+v"(wv[0][2][1]), "+v"(wv[0][3][0]), "+v"(wv[0][3][1]),
                          "+v"(wv[1][0][0]), "+v"(wv[1][0][1]), "+v"(wv[1][1][0]), "+v"(wv[1][1][1]), "+v"(wv[1][2][0]), "+v"(wv[1][2][1]), "+v"(wv[1][3][0]), "+v"(wv[1][3][1]), "+v"(wsp));
#pragma unroll
        for (int e = 0; e < 2; ++e) {
            u32x4 o;
#pragma unroll
            for (int r = 0; r < 4; ++r) o[r] = wv[e][r][0] | (wv[e][r][1] << 16);
            lds_wr16(L_WS + lane * 144 + (2 * u + e) * 16, o);
        }
        WAIT_LGKM0();
        barrier();
        {
            const __amdgpu_buffer_rsrc_t rw = rsrc(ws + WS_WT, 257 * 8192);
#pragma unroll
            for (int e = 0; e < 2; ++e) {
                const int k2l = 16 * u + 8 * e + (lane >> 3);
                const u32x4 o = lds_rd16(L_WS + k2l * 144 + (lane & 7) * 16);
                st_sc1(o, rw, (64 * cq + k2l) * 8192 + ((jv * 2 + jt) * 64 + 16 * jg + 8 * ja + (lane & 7)) * 16);
            }
            if (u == 0 && lane < 16) __builtin_amdgcn_raw_buffer_store_b16((unsigned short)wsp, rw, 256 * 8192 + 2 * b * 16 + lane * 2, 0, 16);
        }
        col_sums(4); col_sums(5); col_sums(6); col_sums(7);
        if (n == 0 && (typeA || u < 2)) *(LAS f32x4*)(L_CS + (16 * u + 4 * g) * 4) = cs;
        lds_wait_ge(SY_XR, 4);  stage_b(IC<0>{});  stage_b(IC<1>{});  stage_b(IC<2>{});  stage_b(IC<3>{});
        lds_wait_ge(SY_XR, 8);  stage_b(IC<4>{});  stage_b(IC<5>{});  stage_b(IC<6>{});  stage_b(IC<7>{});
        lds_wait_ge(SY_XR, 12); stage_b(IC<8>{});  stage_b(IC<9>{});  stage_b(IC<10>{}); stage_b(IC<11>{});
        lds_wait_ge(SY_XR, 16); stage_b(IC<12>{}); stage_b(IC<13>{}); stage_b(IC<14>{}); stage_b(IC<15>{});
    }
    WAIT_LGKM0();
    barrier();
    STAMP(4);
    WAIT_VM(0);
    (void)lds_arrive(SY_BOK, lane);
    const u64 epoch = epoch_finish(ereq);
    if (lds_arrive(SY_AARR, lane) == 7) post_replicas(ws, FL_A, b, lane, epoch);
    lds_wait_ge(SY_BOK, 4);
    STAMP(5);
    {
        f32x4 cs = {0, 0, 0, 0};
        const unsigned swz = (q >> 1) | ((g & 1) << 1);
        const unsigned abase = L_W1B + (((8 * g + q) * 128 + swz * 32 + p * 8) ^ (u << 5));
#pragma unroll
        for (int ks = 0; ks < 16; ++ks) { const unsigned a = abase + ks * 4096; cs = mfma(trread2(a, a + 512), ones, cs); }
        if (n == 0) *(LAS f32x4*)(L_CS + (64 + 16 * u + 4 * g) * 4) = cs;
    }
    WAIT_LGKM0();
    (void)lds_arrive(SY_CS2, lane);
    {
        const unsigned voffw = r8 * 1024 + ((p16 ^ (2 * ((r8 >> 1) & 3))) * 16);
        const char* wb3 = w2 + d0 * 2 + (size_t)u * 32 * 1024;
        const unsigned sd = u * 4096;
        w2_step<0>(wb3, voffw, sd, yrot); w2_step<1>(wb3, voffw, sd, yrot); w2_step<2>(wb3, voffw, sd, yrot); w2_step<3>(wb3, voffw, sd, yrot);
    }
    gen_tw(); gen_c64(); gen_c32();
    if (u != 0) {
        WAIT_VM(0);
        (void)lds_arrive(SY_W2OK, lane);
    }
    STAMP(6);
    __builtin_amdgcn_s_setprio(1);

    const int v = u;
    if (v == 0) {
        poll256<true>(ws, FL_A, b, lane, epoch);
        if (lane == 0) __hip_atomic_store((LAS unsigned*)SY_AOK, 1u, __ATOMIC_RELAXED, __HIP_MEMORY_SCOPE_WORKGROUP);
    } else lds_wait_ge(SY_AOK, 1u);
    STAMP(7); STAMP3(1);
    u32x4 Ur[2][2], Ui[2][2], Wf[2], Wg[2];
    unsigned sync_n = 0;
    {
        const char* xb = ws + WS_X1T + (size_t)(b * 8192 + v * 2048);
        const char* fb = ws + WS_WT + (size_t)(b * 8192 + v * 2048);
        const char* fb2 = ws + WS_WT + (size_t)(256 * 8192 + v * 2048);
        const unsigned vo = lane * 16;
        u32x4 u0, u1;
        GLOAD16(u0, vo, xb, 0); GLOAD16(u1, vo, xb, 1024);
        GLOAD16(Wf[0], vo, fb, 0); GLOAD16(Wf[1], vo, fb, 1024);
        if (b == 0) { GLOAD16(Wg[0], vo, fb2, 0); GLOAD16(Wg[1], vo, fb2, 1024); }
        else { Wg[0] = u32x4{0, 0, 0, 0}; Wg[1] = u32x4{0, 0, 0, 0}; }
        asm volatile("s_waitcnt vmcnt(0)" : "+v"(u0), "+v"(u1), "+v"(Wf[0]), "+v"(Wf[1]), "+v"(Wg[0]), "+v"(Wg[1]) :: "memory");
        const unsigned ta = 8 * v + g, tb = ta + 4;
        lds_wr16(L_ZS + ta * 256 + ((n ^ ta) & 15) * 16, u0);
        lds_wr16(L_ZS + tb * 256 + ((n ^ tb) & 15) * 16, u1);
        sync_n += 4; team_sync(SY_TEAM, sync_n, lane);
#pragma unroll
        for (int mt = 0; mt < 2; ++mt)
#pragma unroll
            for (int ks = 0; ks < 2; ++ks) {
                const unsigned a = L_ZS + (16 * mt + n) * 256;
                Ur[mt][ks] = lds_rd16(a + ((8 * ks + g) ^ n) * 16);
                Ui[mt][ks] = lds_rd16(a + ((8 * ks + 4 + g) ^ n) * 16);
            }
    }
    if (b != 0) p2_unit<true, true>(ws, Ur, Ui, Wf, 2 * b, v, lane, sync_n, C64, S64, N64, C32, S32, N32, tc, tsn);
    else {
        p2_unit<false, true>(ws, Ur, Ur, Wf, 0, v, lane, sync_n, C64, S64, N64, C32, S32, N32, tc, tsn);
        p2_unit<false, false>(ws, Ui, Ui, Wg, 1, v, lane, sync_n, C64, S64, N64, C32, S32, N32, tc, tsn);
    }
    STAMP(8); STAMP3(7);
    WAIT_VM(0);
    if (lds_arrive(SY_ZARR, lane) == 3) post_replicas(ws, FL_Z, b, lane, epoch);
    STAMP(9); STAMP3(9);
    xv = *(const GAS u32x4*)((const GAS char*)x + out_off(tile64, d0, u + 4, lane));
    u32x4 G[4][4];
    g_gen(G[0], d0, u, lane); g_gen(G[1], d0 + 16, u, lane); g_gen(G[2], d0 + 32, u, lane); g_gen(G[3], d0 + 48, u, lane);
    STAMP(10); STAMP3(10);
    if (v == 0) {
        poll256<false>(ws, FL_Z, b, lane, epoch);
        if (lane == 0) {
            __hip_atomic_store((LAS unsigned*)SY_ZOK, 1u, __ATOMIC_RELAXED, __HIP_MEMORY_SCOPE_WORKGROUP);
            if (b == 0) __hip_atomic_store((GAS u64*)(ws + WS_FLAG), epoch, __ATOMIC_RELAXED, __HIP_MEMORY_SCOPE_AGENT);
        }
    } else lds_wait_ge(SY_ZOK, 1u);
    lds_wait_ge(SY_Y6, 4);
    STAMP(11); STAMP3(11);
    const unsigned zreg = L_Z + v * 16384;
    {
        const __amdgpu_buffer_rsrc_t rz = rsrc(ws + WS_ZT, 512 * 4096);
        const int r8 = lane >> 3, ch = lane & 7;
        u32x4 z[16];
#pragma unroll
        for (int i = 0; i < 16; ++i) z[i] = ld_sc1(rz, (128 * v + 8 * i + r8) * 4096 + tile64 * 128 + ch * 16);
#pragma unroll
        for (int i = 0; i < 16; ++i) lds_wr16(zreg + (8 * i + r8) * 128 + ((ch ^ (4 * ((r8 >> 1) & 1))) * 16), z[i]);
    }
    STAMP(12); STAMP3(12);
    f32x4 acc[4][4];
    {
        auto rdZ = [&](int j) -> u32x4 {
            const int ks = j >> 2, t = j & 3;
            const unsigned a = zreg + (32 * ks + 8 * g + q) * 128 + (((t >> 1) ^ ((q >> 1) & 1)) * 64) + p * 16 + (t & 1) * 8;
            return trread2(a, a + 512);
        };
        u32x4 z[4];
#pragma unroll
        for (int j = 0; j < 4; ++j) z[j] = rdZ(j);
        __builtin_amdgcn_sched_barrier(0);
#pragma unroll
        for (int j = 0; j < 16; ++j) {
#pragma unroll
            for (int db = 0; db < 4; ++db) mfma_ip(acc[db][j & 3], G[db][j >> 2], z[j & 3], j < 4);
            if (j + 4 < 16) z[j & 3] = rdZ(j + 4);
            __builtin_amdgcn_sched_barrier(0);
        }
    }
    MFMA_DONE();
#pragma unroll
    for (int t = 0; t < 4; ++t)
#pragma unroll
        for (int db = 0; db < 4; ++db) PIN4(acc[db][t]);
#pragma unroll
    for (int t = 0; t < 4; ++t)
#pragma unroll
        for (int db = 0; db < 4; ++db) *(LAS f32x4*)(zreg + (t * 4 + db) * 1024 + lane * 16) = acc[db][t];
    __builtin_amdgcn_s_setprio(0);
    STAMP(13); STAMP3(13);
}

#if defined(__HIP_DEVICE_COMPILE__)
#define NOPK __attribute__((target("no-packed-fp32-ops")))
#else
#define NOPK
#endif
__global__ NOPK void __launch_bounds__(512, 2) fused_kernel(const char* x, const unsigned short* wr, const unsigned short* wi,
                                                        const char* __restrict__ w1, const char* __restrict__ w2, char* ws,
                                                        char* __restrict__ out) {
    const int b = blockIdx.x, lane = threadIdx.x & 63, w = __builtin_amdgcn_readfirstlane(threadIdx.x >> 6);
    STAMP(0);
    if (w >= 4) STAMP3(0);
    const int xcd = b & 7, slot = b >> 3;
    if (w == 7 || w == 3) {
        unsigned long long pc;
        asm volatile("s_getpc_b64 %0" : "=s"(pc));
        const unsigned piece = (unsigned)(2 * slot + (w >> 2)) % CODE_PIECES;
        const unsigned voff = piece * 4096 + lane * 64;
        asm volatile("s_mov_b32 m0, %2\n\ts_nop 0\n\tglobal_load_lds_dword %0, %1" :: "v"(voff), "s"(pc), "s"(L_CS) : "memory");
    }
    const int hf = xcd & 1, P = 4 * (xcd >> 1) + (slot >> 3), i = slot & 7;
    const bool typeA = i < 4;
    const int col0 = 896 * hf + (typeA ? 128 * i : 512 + 96 * (i - 4));
    const int fg0 = 16 * hf + (typeA ? i : 4 + 3 * (i - 4));
    const int rot = (i + 2 * (slot >> 3)) & 7;
    const int tile64 = 4 * xcd + (slot >> 3), d0 = 64 * (slot & 7);
    const unsigned yrot = (unsigned)(((7 * (slot & 7)) >> 2) + 3 * (slot >> 3)) % 14u;
    if (threadIdx.x < 16) *(LAS unsigned*)(unsigned)(L_SY + threadIdx.x * 4) = 0u;
    u32x4 xv;
    if (w < 4) m_team(x, w2, ws, b, P, typeA, col0 >> 5, fg0, 8 * hf + i, tile64, d0, rot, yrot, w, lane, xv);
    else s_team(x, w1, w2, wr, wi, ws, b, typeA, col0, fg0, tile64, d0, rot, yrot, w - 4, lane, xv);
    WAIT_LGKM0();
    barrier();
    STAMP(14);
    int lane2 = lane;
    asm volatile("" : "+v"(lane2));
    const int n = lane2 & 15, g = lane2 >> 4;
    const int T = w & 3, dp = w >> 2;
    float y[8], s[8];
#pragma unroll
    for (int e = 0; e < 2; ++e) {
        const unsigned a = (2 * dp + (g >> 1)) * 1024 + (n + 16 * (2 * (g & 1) + e)) * 16;
        f32x4 py[4], ps[4];
#pragma unroll
        for (int k = 0; k < 4; ++k) { py[k] = *(LAS f32x4*)(a + (T * 4 + k) * 4096); ps[k] = *(LAS f32x4*)(a + L_Z + k * 16384 + T * 4096); }
#pragma unroll
        for (int r = 0; r < 4; ++r) {
            y[4 * e + r] = (py[0][r] + py[1][r]) + (py[2][r] + py[3][r]);
            s[4 * e + r] = (ps[0][r] + ps[1][r]) + (ps[2][r] + ps[3][r]);
        }
    }
    float o[8];
#pragma unroll
    for (int j = 0; j < 8; ++j) {
        const float yb = bf_round(y[j]);
        const float gg = bf_round(yb * __builtin_amdgcn_rcpf(1.0f + __expf(-yb)));
        const float sb = bf_round(s[j] * (2.0f / 512.0f));
        const float xx = (j & 1) ? bf_hi(xv[j >> 1]) : bf_lo(xv[j >> 1]);
        o[j] = xx + bf_round(sb * gg);
    }
    const u32x4 ov = {pk2(o[0], o[1]), pk2(o[2], o[3]), pk2(o[4], o[5]), pk2(o[6], o[7])};
    __builtin_amdgcn_raw_buffer_store_b128(ov, rsrc(out, 2097152), out_off(tile64, d0, w, lane), 0, 17);
    STAMP(15);
}

extern "C" void kernel_launch(void* const* d_in, const int* in_sizes, int n_in, void* d_out, int out_size, void* d_ws,
                              size_t ws_size, hipStream_t stream) {
    (void)in_sizes; (void)n_in; (void)out_size; (void)ws_size;
    (void)hipFuncSetAttribute((const void*)fused_kernel, hipFuncAttributeMaxDynamicSharedMemorySize, L_END);
    fused_kernel<<<256, 512, L_END, stream>>>((const char*)d_in[0], (const unsigned short*)d_in[1], (const unsigned short*)d_in[2],
                                              (const char*)d_in[4], (const char*)d_in[5], (char*)d_ws, (char*)d_out);
}
```
